```python
import torch

def _softmax(x, axis=-1):
    return torch.softmax(x, dim=axis)

def _gelu(x):
    return torch.nn.functional.gelu(x, approximate='tanh')

def _silu(x):
    return torch.nn.functional.silu(x)

def _sigmoid(x):
    return torch.sigmoid(x)

def _layernorm(x, axis=-1, eps=1e-05):
    m = x.mean(dim=axis, keepdim=True)
    v = x.var(dim=axis, keepdim=True, unbiased=False)
    return (x - m) / torch.sqrt(v + eps)

def _scatter_add(tgt, idx, src, axis=0):
    return tgt.clone().index_add_(axis, idx, src)

def _segment_sum(src, seg, n):
    out = torch.zeros((n,) + src.shape[1:], dtype=src.dtype)
    return out.index_add_(0, seg, src)

def _pad(x, pw):
    flat = tuple((v for pair in reversed(pw) for v in pair))
    return torch.nn.functional.pad(x, flat)
kernel_name = 'standalone__tree_speculative_verify_tree_attention_over_candidate_token_tree__sampling'

def setup_inputs():

    _w = lambda a: (lambda t: t.to(torch.bfloat16) if t.is_floating_point() else t)(torch.as_tensor(a))
    B, Hq, Hkv, T, C, D = (8, 32, 8, 1, 8192, 128)
    tree_mask = torch.zeros((B, T, T), dtype=float)
    for b in range(B):
        parent = torch.zeros(T, dtype=torch.int64)
        for i in range(1, T):
            parent[i] = int(torch.randint(0, i, (1,)).item())
        for i in range(T):
            j = i
            tree_mask[b, i, j] = 1.0
            while j > 0:
                j = parent[j]
                tree_mask[b, i, j] = 1.0
    return {'q': _w((lambda *s: torch.randn(*s, dtype=torch.bfloat16) * 0.8)(B, Hq, T, D)), 'k_cache': _w((lambda *s: torch.randn(*s, dtype=torch.bfloat16) * 0.8)(B, Hkv, C, D)), 'v_cache': _w((lambda *s: torch.randn(*s, dtype=torch.bfloat16) * 0.05)(B, Hkv, C, D)), 'k_tree': _w((lambda *s: torch.randn(*s, dtype=torch.bfloat16) * 0.8)(B, Hkv, T, D)), 'v_tree': _w((lambda *s: torch.randn(*s, dtype=torch.bfloat16) * 0.05)(B, Hkv, T, D)), 'tree_mask': _w(tree_mask)}

def reference(q, k_cache, v_cache, k_tree, v_tree, tree_mask):
    B, Hq, T, D = q.shape
    Hkv, C = (k_cache.shape[1], k_cache.shape[2])
    rep = Hq // Hkv
    kc = (lambda x, n, axis: torch.repeat_interleave(x, n, dim=axis))(k_cache, rep, axis=1)
    vc = (lambda x, n, axis: torch.repeat_interleave(x, n, dim=axis))(v_cache, rep, axis=1)
    kt = (lambda x, n, axis: torch.repeat_interleave(x, n, dim=axis))(k_tree, rep, axis=1)
    vt = (lambda x, n, axis: torch.repeat_interleave(x, n, dim=axis))(v_tree, rep, axis=1)
    k = torch.concatenate([kc, kt], axis=2)
    v = torch.concatenate([vc, vt], axis=2)
    scale = D ** (-0.5)
    scores = torch.einsum('bhqd,bhkd->bhqk', q, k) * scale
    ctx_mask = torch.ones((B, 1, T, C), dtype=scores.dtype)
    tm = tree_mask[:, None, :, :].clone()
    full_mask = torch.concatenate([ctx_mask, tm], axis=-1)
    scores = scores + (1.0 - full_mask) * -1000000000.0
    p = _softmax(scores, axis=-1)
    return torch.einsum('bhqk,bhkd->bhqd', p, v)
```

```cpp
#include <hip/hip_runtime.h>
#include <hip/hip_bf16.h>
#include <cstdint>

#define NSPLIT 4
#define NWAVE 8
#define KV_NT 1

constexpr int B_ = 8, HQ = 32, HKV = 8, CTX = 8192, D = 128, G = HQ / HKV;
constexpr int NTHR = NWAVE * 64;
constexpr int KEYS_PER_WG = CTX / NSPLIT;
constexpr int TILES_PER_WAVE = KEYS_PER_WG / 32 / NWAVE;
static_assert(TILES_PER_WAVE >= 2 && TILES_PER_WAVE % 2 == 0, "tiles");
constexpr float SCALE = 0.088388347648318440f;
constexpr float LOG2E = 1.4426950408889634f;

typedef unsigned u32x4 __attribute__((ext_vector_type(4)));
typedef float f32x4 __attribute__((ext_vector_type(4)));
typedef __bf16 bf16x8 __attribute__((ext_vector_type(8)));
typedef __bf16 bf16x2 __attribute__((ext_vector_type(2)));
typedef float f32x2 __attribute__((ext_vector_type(2)));

__device__ __forceinline__ u32x4 ld16(const unsigned char* p) {
  return __builtin_nontemporal_load(reinterpret_cast<const u32x4*>(p));
}
__device__ __forceinline__ f32x4 mfma(u32x4 a, u32x4 b, f32x4 c) {
  return __builtin_amdgcn_mfma_f32_16x16x32_bf16(__builtin_bit_cast(bf16x8, a), __builtin_bit_cast(bf16x8, b), c, 0, 0, 0);
}
template <int CTRL> __device__ __forceinline__ float dpp0(float x) {
  return __builtin_bit_cast(float, __builtin_amdgcn_update_dpp(0, __builtin_bit_cast(int, x), CTRL, 0xf, 0xf, true));
}
__device__ __forceinline__ unsigned cvtpk(float lo, float hi) {
  f32x2 v = {lo, hi};
  bf16x2 r = __builtin_convertvector(v, bf16x2);
  return __builtin_bit_cast(unsigned, r);
}
__device__ __forceinline__ float xrow_max(float x) {
  auto s = __builtin_amdgcn_permlane16_swap(__float_as_uint(x), __float_as_uint(x), false, false);
  x = fmaxf(__uint_as_float(s[0]), __uint_as_float(s[1]));
  auto t = __builtin_amdgcn_permlane32_swap(__float_as_uint(x), __float_as_uint(x), false, false);
  return fmaxf(__uint_as_float(t[0]), __uint_as_float(t[1]));
}
__device__ __forceinline__ float xrow_sum(float x) {
  auto s = __builtin_amdgcn_permlane16_swap(__float_as_uint(x), __float_as_uint(x), false, false);
  x = __uint_as_float(s[0]) + __uint_as_float(s[1]);
  auto t = __builtin_amdgcn_permlane32_swap(__float_as_uint(x), __float_as_uint(x), false, false);
  return __uint_as_float(t[0]) + __uint_as_float(t[1]);
}

struct State { float m, l; f32x4 acc[8]; };

__device__ __forceinline__ void qk(const u32x4 (&K)[8], u32x4 qf, float (&s)[8]) {
#pragma unroll
  for (int i = 0; i < 8; ++i) {
    f32x4 z = {0.f, 0.f, 0.f, 0.f};
    f32x4 d = mfma(K[i], qf, z);
    s[i] = ((d[0] + dpp0<0x104>(d[1])) + dpp0<0x108>(d[2])) + dpp0<0x10C>(d[3]);
  }
}
__device__ __forceinline__ u32x4 softmax_step(float (&s)[8], State& st) {
  constexpr float C2 = SCALE * LOG2E;
  float mx = fmaxf(fmaxf(fmaxf(s[0], s[1]), fmaxf(s[2], s[3])), fmaxf(fmaxf(s[4], s[5]), fmaxf(s[6], s[7])));
  mx = xrow_max(mx) * C2;
  const float mn = fmaxf(st.m, mx);
  const float alpha = __builtin_amdgcn_exp2f(st.m - mn);
  st.m = mn;
  float p[8], sum = 0.f;
#pragma unroll
  for (int i = 0; i < 8; ++i) { p[i] = __builtin_amdgcn_exp2f(fmaf(s[i], C2, -mn)); sum += p[i]; }
  st.l = fmaf(st.l, alpha, sum);
#pragma unroll
  for (int e = 0; e < 8; ++e) st.acc[e] *= alpha;
  u32x4 pf = {cvtpk(p[0], p[1]), cvtpk(p[2], p[3]), cvtpk(p[4], p[5]), cvtpk(p[6], p[7])};
  return pf;
}
__device__ __forceinline__ void pv(const u32x4 (&V)[8], u32x4 pf, State& st) {
#pragma unroll
  for (int e = 0; e < 8; ++e) {
    const unsigned sel = (e & 1) ? 0x07060302u : 0x05040100u;
    u32x4 vf;
#pragma unroll
    for (int t = 0; t < 4; ++t) vf[t] = __builtin_amdgcn_perm(V[2 * t + 1][e >> 1], V[2 * t][e >> 1], sel);
    st.acc[e] = mfma(vf, pf, st.acc[e]);
  }
}

__global__ void __launch_bounds__(NTHR, NWAVE / 4)
attn_split(const unsigned char* __restrict__ q, const unsigned char* __restrict__ kc, const unsigned char* __restrict__ vc,
           const unsigned char* __restrict__ kt, const unsigned char* __restrict__ vt, const unsigned short* __restrict__ mask,
           float* __restrict__ ws) {
  __shared__ __attribute__((aligned(16))) float lds[NWAVE * G * (D + 4)];
  const int bh = blockIdx.x / NSPLIT, split = blockIdx.x % NSPLIT;
  const int tid = threadIdx.x, lane = tid & 63, wave = __builtin_amdgcn_readfirstlane(tid >> 6);
  const int g = lane >> 4, c = lane & 15;
  const size_t base = ((size_t)bh * CTX + (size_t)split * KEYS_PER_WG) * (D * 2) + (size_t)wave * 8192;
  const unsigned char* kp = kc + base + (c >> 2) * 256 + g * 64 + (c & 3) * 16;
  const unsigned char* vp = vc + base + lane * 16;
  constexpr size_t TSTRIDE = (size_t)NWAVE * 8192;

  u32x4 KA[8], VA[8], KB[8], VB[8];
#pragma unroll
  for (int i = 0; i < 8; ++i) KA[i] = ld16(kp + i * 1024);
  const u32x4 qf = *reinterpret_cast<const u32x4*>(q + ((size_t)bh * G + (c & 3)) * (D * 2) + ((c >> 2) + 4 * g) * 16);
#pragma unroll
  for (int i = 0; i < 8; ++i) VA[i] = ld16(vp + i * 1024);
#pragma unroll
  for (int i = 0; i < 8; ++i) KB[i] = ld16(kp + TSTRIDE + i * 1024);
#pragma unroll
  for (int i = 0; i < 8; ++i) VB[i] = ld16(vp + TSTRIDE + i * 1024);
  __builtin_amdgcn_sched_barrier(0);

  State st;
  st.m = -3.4028234e38f; st.l = 0.f;
#pragma unroll
  for (int e = 0; e < 8; ++e) st.acc[e] = f32x4{0.f, 0.f, 0.f, 0.f};

  float s[8];
#pragma unroll 1
  for (int t = 0; t < TILES_PER_WAVE; t += 2) {
    const bool more = (t + 2 < TILES_PER_WAVE);
    const size_t off = more ? (size_t)(t + 2) * TSTRIDE : (size_t)t * TSTRIDE;
    qk(KA, qf, s);
    __builtin_amdgcn_sched_barrier(0);
    if (more) {
#pragma unroll
      for (int i = 0; i < 8; ++i) KA[i] = ld16(kp + off + i * 1024);
    }
    __builtin_amdgcn_sched_barrier(0);
    u32x4 pf = softmax_step(s, st);
    pv(VA, pf, st);
    __builtin_amdgcn_sched_barrier(0);
    if (more) {
#pragma unroll
      for (int i = 0; i < 8; ++i) VA[i] = ld16(vp + off + i * 1024);
    }
    __builtin_amdgcn_sched_barrier(0);
    qk(KB, qf, s);
    __builtin_amdgcn_sched_barrier(0);
    if (more) {
#pragma unroll
      for (int i = 0; i < 8; ++i) KB[i] = ld16(kp + off + TSTRIDE + i * 1024);
    }
    __builtin_amdgcn_sched_barrier(0);
    pf = softmax_step(s, st);
    pv(VB, pf, st);
    __builtin_amdgcn_sched_barrier(0);
    if (more) {
#pragma unroll
      for (int i = 0; i < 8; ++i) VB[i] = ld16(vp + off + TSTRIDE + i * 1024);
    }
    __builtin_amdgcn_sched_barrier(0);
  }
  const float lsum = xrow_sum(st.l);
  if (c < G) {
    float* rec = lds + (wave * G + c) * (D + 4);
#pragma unroll
    for (int r = 0; r < 4; ++r) {
      *reinterpret_cast<f32x4*>(rec + 4 + 32 * g + 8 * r) = f32x4{st.acc[0][r], st.acc[1][r], st.acc[2][r], st.acc[3][r]};
      *reinterpret_cast<f32x4*>(rec + 4 + 32 * g + 8 * r + 4) = f32x4{st.acc[4][r], st.acc[5][r], st.acc[6][r], st.acc[7][r]};
    }
    if (g == 0) { rec[0] = st.m; rec[1] = lsum; }
  }
  __syncthreads();
  if (tid < G * 64) {
    const int h = tid >> 6, d0 = 2 * lane;
    float M = -3.4028234e38f;
#pragma unroll
    for (int w = 0; w < NWAVE; ++w) M = fmaxf(M, lds[(w * G + h) * (D + 4)]);
    float L = 0.f, o0 = 0.f, o1 = 0.f;
#pragma unroll
    for (int w = 0; w < NWAVE; ++w) {
      const float* rec = lds + (w * G + h) * (D + 4);
      const float wt = __builtin_amdgcn_exp2f(rec[0] - M);
      L = fmaf(wt, rec[1], L); o0 = fmaf(wt, rec[4 + d0], o0); o1 = fmaf(wt, rec[5 + d0], o1);
    }
    float* part = ws + ((size_t)(bh * G + h) * NSPLIT + split) * (D + 4);
    *reinterpret_cast<float2*>(part + 4 + d0) = make_float2(o0, o1);
    if (lane == 0) *reinterpret_cast<float2*>(part) = make_float2(M, L);
  }
}

__device__ __forceinline__ float bf2f(unsigned short x) { return __uint_as_float((unsigned)x << 16); }

__global__ void __launch_bounds__(64)
attn_combine(const unsigned short* __restrict__ q, const unsigned short* __restrict__ kt, const unsigned short* __restrict__ vt,
             const unsigned short* __restrict__ mask, const float* __restrict__ ws, unsigned* __restrict__ out) {
  const int row = blockIdx.x, lane = threadIdx.x, bh = row / G, b = bh / HKV, d0 = 2 * lane;
  const unsigned qq = *reinterpret_cast<const unsigned*>(q + (size_t)row * D + d0);
  const unsigned kk = *reinterpret_cast<const unsigned*>(kt + (size_t)bh * D + d0);
  const unsigned vv = *reinterpret_cast<const unsigned*>(vt + (size_t)bh * D + d0);
  const float mk = bf2f(mask[b]);
  float2 ml[NSPLIT], o[NSPLIT];
#pragma unroll
  for (int s = 0; s < NSPLIT; ++s) {
    const float* part = ws + ((size_t)row * NSPLIT + s) * (D + 4);
    ml[s] = *reinterpret_cast<const float2*>(part);
    o[s] = *reinterpret_cast<const float2*>(part + 4 + d0);
  }
  float dot = __uint_as_float(qq << 16) * __uint_as_float(kk << 16) + __uint_as_float(qq & 0xffff0000u) * __uint_as_float(kk & 0xffff0000u);
#pragma unroll
  for (int off = 32; off >= 1; off >>= 1) dot += __shfl_xor(dot, off, 64);
  const float tt = (dot * SCALE + (1.0f - mk) * -1.0e9f) * LOG2E;
  float M = tt;
#pragma unroll
  for (int s = 0; s < NSPLIT; ++s) M = fmaxf(M, ml[s].x);
  const float wt0 = __builtin_amdgcn_exp2f(tt - M);
  float L = wt0, o0 = wt0 * __uint_as_float(vv << 16), o1 = wt0 * __uint_as_float(vv & 0xffff0000u);
#pragma unroll
  for (int s = 0; s < NSPLIT; ++s) {
    const float wt = __builtin_amdgcn_exp2f(ml[s].x - M);
    L = fmaf(wt, ml[s].y, L); o0 = fmaf(wt, o[s].x, o0); o1 = fmaf(wt, o[s].y, o1);
  }
  const float inv = 1.0f / L;
  out[(size_t)row * (D / 2) + lane] = cvtpk(o0 * inv, o1 * inv);
}

extern "C" void kernel_launch(void* const* d_in, const int* in_sizes, int n_in, void* d_out, int out_size, void* d_ws,
                              size_t ws_size, hipStream_t stream) {
  (void)in_sizes; (void)n_in; (void)out_size; (void)ws_size;
  attn_split<<<B_ * HKV * NSPLIT, NTHR, 0, stream>>>((const unsigned char*)d_in[0], (const unsigned char*)d_in[1], (const unsigned char*)d_in[2],
                                                      (const unsigned char*)d_in[3], (const unsigned char*)d_in[4],
                                                      (const unsigned short*)d_in[5], (float*)d_ws);
  attn_combine<<<B_ * HQ, 64, 0, stream>>>((const unsigned short*)d_in[0], (const unsigned short*)d_in[3], (const unsigned short*)d_in[4],
                                           (const unsigned short*)d_in[5], (const float*)d_ws, (unsigned*)d_out);
}
```

```cpp
#include <hip/hip_runtime.h>
#include <hip/hip_bf16.h>
#include <cstdint>

#define NSPLIT 4
#define NWAVE 4
#define KV_NT 1

constexpr int B_ = 8, HQ = 32, HKV = 8, CTX = 8192, D = 128, G = HQ / HKV;
constexpr int NTHR = NWAVE * 64;
constexpr int KEYS_PER_WG = CTX / NSPLIT;
constexpr int TILES_PER_WAVE = KEYS_PER_WG / 32 / NWAVE;
static_assert(TILES_PER_WAVE >= 2 && TILES_PER_WAVE % 2 == 0, "tiles");
constexpr float SCALE = 0.088388347648318440f;
constexpr float LOG2E = 1.4426950408889634f;

typedef unsigned u32x4 __attribute__((ext_vector_type(4)));
typedef float f32x4 __attribute__((ext_vector_type(4)));
typedef __bf16 bf16x8 __attribute__((ext_vector_type(8)));
typedef __bf16 bf16x2 __attribute__((ext_vector_type(2)));
typedef float f32x2 __attribute__((ext_vector_type(2)));

__device__ __forceinline__ u32x4 ld16(const unsigned char* p) {
  return __builtin_nontemporal_load(reinterpret_cast<const u32x4*>(p));
}
__device__ __forceinline__ u32x4 ldv16(const unsigned char* p) { return *reinterpret_cast<const u32x4*>(p); }
__device__ __forceinline__ f32x4 mfma(u32x4 a, u32x4 b, f32x4 c) {
  return __builtin_amdgcn_mfma_f32_16x16x32_bf16(__builtin_bit_cast(bf16x8, a), __builtin_bit_cast(bf16x8, b), c, 0, 0, 0);
}
template <int CTRL> __device__ __forceinline__ float dpp0(float x) {
  return __builtin_bit_cast(float, __builtin_amdgcn_update_dpp(0, __builtin_bit_cast(int, x), CTRL, 0xf, 0xf, true));
}
__device__ __forceinline__ unsigned cvtpk(float lo, float hi) {
  f32x2 v = {lo, hi};
  bf16x2 r = __builtin_convertvector(v, bf16x2);
  return __builtin_bit_cast(unsigned, r);
}
__device__ __forceinline__ float xrow_max(float x) {
  auto s = __builtin_amdgcn_permlane16_swap(__float_as_uint(x), __float_as_uint(x), false, false);
  x = fmaxf(__uint_as_float(s[0]), __uint_as_float(s[1]));
  auto t = __builtin_amdgcn_permlane32_swap(__float_as_uint(x), __float_as_uint(x), false, false);
  return fmaxf(__uint_as_float(t[0]), __uint_as_float(t[1]));
}
__device__ __forceinline__ float xrow_sum(float x) {
  auto s = __builtin_amdgcn_permlane16_swap(__float_as_uint(x), __float_as_uint(x), false, false);
  x = __uint_as_float(s[0]) + __uint_as_float(s[1]);
  auto t = __builtin_amdgcn_permlane32_swap(__float_as_uint(x), __float_as_uint(x), false, false);
  return __uint_as_float(t[0]) + __uint_as_float(t[1]);
}

struct State { float m, l; f32x4 acc[8]; };

__device__ __forceinline__ void qk(const u32x4 (&K)[8], u32x4 qf, float (&s)[8]) {
#pragma unroll
  for (int i = 0; i < 8; ++i) {
    f32x4 z = {0.f, 0.f, 0.f, 0.f};
    f32x4 d = mfma(K[i], qf, z);
    s[i] = ((d[0] + dpp0<0x104>(d[1])) + dpp0<0x108>(d[2])) + dpp0<0x10C>(d[3]);
  }
}
__device__ __forceinline__ u32x4 softmax_step(float (&s)[8], State& st) {
  constexpr float C2 = SCALE * LOG2E;
  float mx = fmaxf(fmaxf(fmaxf(s[0], s[1]), fmaxf(s[2], s[3])), fmaxf(fmaxf(s[4], s[5]), fmaxf(s[6], s[7])));
  mx = xrow_max(mx) * C2;
  const float mn = fmaxf(st.m, mx);
  const float alpha = __builtin_amdgcn_exp2f(st.m - mn);
  st.m = mn;
  float p[8], sum = 0.f;
#pragma unroll
  for (int i = 0; i < 8; ++i) { p[i] = __builtin_amdgcn_exp2f(fmaf(s[i], C2, -mn)); sum += p[i]; }
  st.l = fmaf(st.l, alpha, sum);
#pragma unroll
  for (int e = 0; e < 8; ++e) st.acc[e] *= alpha;
  u32x4 pf = {cvtpk(p[0], p[1]), cvtpk(p[2], p[3]), cvtpk(p[4], p[5]), cvtpk(p[6], p[7])};
  return pf;
}
__device__ __forceinline__ void pv(const u32x4 (&V)[8], u32x4 pf, State& st) {
#pragma unroll
  for (int e = 0; e < 8; ++e) {
    const unsigned sel = (e & 1) ? 0x07060302u : 0x05040100u;
    u32x4 vf;
#pragma unroll
    for (int t = 0; t < 4; ++t) vf[t] = __builtin_amdgcn_perm(V[2 * t + 1][e >> 1], V[2 * t][e >> 1], sel);
    st.acc[e] = mfma(vf, pf, st.acc[e]);
  }
}

__global__ void __launch_bounds__(NTHR, NWAVE / 4)
attn_split(const unsigned char* __restrict__ q, const unsigned char* __restrict__ kc, const unsigned char* __restrict__ vc,
           const unsigned char* __restrict__ kt, const unsigned char* __restrict__ vt, const unsigned short* __restrict__ mask,
           float* __restrict__ ws) {
  __shared__ __attribute__((aligned(16))) float lds[NWAVE * G * (D + 4)];
  const int bh = blockIdx.x / NSPLIT, split = blockIdx.x % NSPLIT;
  const int tid = threadIdx.x, lane = tid & 63, wave = __builtin_amdgcn_readfirstlane(tid >> 6);
  const int g = lane >> 4, c = lane & 15;
  const size_t base = ((size_t)bh * CTX + (size_t)split * KEYS_PER_WG) * (D * 2) + (size_t)wave * 8192;
  const unsigned char* kp = kc + base + (c >> 2) * 256 + g * 64 + (c & 3) * 16;
  const unsigned char* vp = vc + base + lane * 16;
  constexpr size_t TSTRIDE = (size_t)NWAVE * 8192;

  u32x4 KA[8], VA[8], KB[8], VB[8];
#pragma unroll
  for (int i = 0; i < 8; ++i) KA[i] = ld16(kp + i * 1024);
  const u32x4 qf = *reinterpret_cast<const u32x4*>(q + ((size_t)bh * G + (c & 3)) * (D * 2) + ((c >> 2) + 4 * g) * 16);
#pragma unroll
  for (int i = 0; i < 8; ++i) VA[i] = ldv16(vp + i * 1024);
#pragma unroll
  for (int i = 0; i < 8; ++i) KB[i] = ld16(kp + TSTRIDE + i * 1024);
#pragma unroll
  for (int i = 0; i < 8; ++i) VB[i] = ldv16(vp + TSTRIDE + i * 1024);
  __builtin_amdgcn_sched_barrier(0);

  State st;
  st.m = -3.4028234e38f; st.l = 0.f;
#pragma unroll
  for (int e = 0; e < 8; ++e) st.acc[e] = f32x4{0.f, 0.f, 0.f, 0.f};

  float s[8];
#pragma unroll 1
  for (int t = 0; t < TILES_PER_WAVE; t += 2) {
    const bool more = (t + 2 < TILES_PER_WAVE);
    const size_t off = more ? (size_t)(t + 2) * TSTRIDE : (size_t)t * TSTRIDE;
    qk(KA, qf, s);
    __builtin_amdgcn_sched_barrier(0);
    if (more) {
#pragma unroll
      for (int i = 0; i < 8; ++i) KA[i] = ld16(kp + off + i * 1024);
    }
    __builtin_amdgcn_sched_barrier(0);
    u32x4 pf = softmax_step(s, st);
    pv(VA, pf, st);
    __builtin_amdgcn_sched_barrier(0);
    if (more) {
#pragma unroll
      for (int i = 0; i < 8; ++i) VA[i] = ldv16(vp + off + i * 1024);
    }
    __builtin_amdgcn_sched_barrier(0);
    qk(KB, qf, s);
    __builtin_amdgcn_sched_barrier(0);
    if (more) {
#pragma unroll
      for (int i = 0; i < 8; ++i) KB[i] = ld16(kp + off + TSTRIDE + i * 1024);
    }
    __builtin_amdgcn_sched_barrier(0);
    pf = softmax_step(s, st);
    pv(VB, pf, st);
    __builtin_amdgcn_sched_barrier(0);
    if (more) {
#pragma unroll
      for (int i = 0; i < 8; ++i) VB[i] = ldv16(vp + off + TSTRIDE + i * 1024);
    }
    __builtin_amdgcn_sched_barrier(0);
  }
  const float lsum = xrow_sum(st.l);
  if (c < G) {
    float* rec = lds + (wave * G + c) * (D + 4);
#pragma unroll
    for (int r = 0; r < 4; ++r) {
      *reinterpret_cast<f32x4*>(rec + 4 + 32 * g + 8 * r) = f32x4{st.acc[0][r], st.acc[1][r], st.acc[2][r], st.acc[3][r]};
      *reinterpret_cast<f32x4*>(rec + 4 + 32 * g + 8 * r + 4) = f32x4{st.acc[4][r], st.acc[5][r], st.acc[6][r], st.acc[7][r]};
    }
    if (g == 0) { rec[0] = st.m; rec[1] = lsum; }
  }
  __syncthreads();
  if (tid < G * 64) {
    const int h = tid >> 6, d0 = 2 * lane;
    float M = -3.4028234e38f;
#pragma unroll
    for (int w = 0; w < NWAVE; ++w) M = fmaxf(M, lds[(w * G + h) * (D + 4)]);
    float L = 0.f, o0 = 0.f, o1 = 0.f;
#pragma unroll
    for (int w = 0; w < NWAVE; ++w) {
      const float* rec = lds + (w * G + h) * (D + 4);
      const float wt = __builtin_amdgcn_exp2f(rec[0] - M);
      L = fmaf(wt, rec[1], L); o0 = fmaf(wt, rec[4 + d0], o0); o1 = fmaf(wt, rec[5 + d0], o1);
    }
    float* part = ws + ((size_t)(bh * G + h) * NSPLIT + split) * (D + 4);
    *reinterpret_cast<float2*>(part + 4 + d0) = make_float2(o0, o1);
    if (lane == 0) *reinterpret_cast<float2*>(part) = make_float2(M, L);
  }
}

__device__ __forceinline__ float bf2f(unsigned short x) { return __uint_as_float((unsigned)x << 16); }

__global__ void __launch_bounds__(64)
attn_combine(const unsigned short* __restrict__ q, const unsigned short* __restrict__ kt, const unsigned short* __restrict__ vt,
             const unsigned short* __restrict__ mask, const float* __restrict__ ws, unsigned* __restrict__ out) {
  const int row = blockIdx.x, lane = threadIdx.x, bh = row / G, b = bh / HKV, d0 = 2 * lane;
  const unsigned qq = *reinterpret_cast<const unsigned*>(q + (size_t)row * D + d0);
  const unsigned kk = *reinterpret_cast<const unsigned*>(kt + (size_t)bh * D + d0);
  const unsigned vv = *reinterpret_cast<const unsigned*>(vt + (size_t)bh * D + d0);
  const float mk = bf2f(mask[b]);
  float2 ml[NSPLIT], o[NSPLIT];
#pragma unroll
  for (int s = 0; s < NSPLIT; ++s) {
    const float* part = ws + ((size_t)row * NSPLIT + s) * (D + 4);
    ml[s] = *reinterpret_cast<const float2*>(part);
    o[s] = *reinterpret_cast<const float2*>(part + 4 + d0);
  }
  float dot = __uint_as_float(qq << 16) * __uint_as_float(kk << 16) + __uint_as_float(qq & 0xffff0000u) * __uint_as_float(kk & 0xffff0000u);
#pragma unroll
  for (int off = 32; off >= 1; off >>= 1) dot += __shfl_xor(dot, off, 64);
  const float tt = (dot * SCALE + (1.0f - mk) * -1.0e9f) * LOG2E;
  float M = tt;
#pragma unroll
  for (int s = 0; s < NSPLIT; ++s) M = fmaxf(M, ml[s].x);
  const float wt0 = __builtin_amdgcn_exp2f(tt - M);
  float L = wt0, o0 = wt0 * __uint_as_float(vv << 16), o1 = wt0 * __uint_as_float(vv & 0xffff0000u);
#pragma unroll
  for (int s = 0; s < NSPLIT; ++s) {
    const float wt = __builtin_amdgcn_exp2f(ml[s].x - M);
    L = fmaf(wt, ml[s].y, L); o0 = fmaf(wt, o[s].x, o0); o1 = fmaf(wt, o[s].y, o1);
  }
  const float inv = 1.0f / L;
  out[(size_t)row * (D / 2) + lane] = cvtpk(o0 * inv, o1 * inv);
}

extern "C" void kernel_launch(void* const* d_in, const int* in_sizes, int n_in, void* d_out, int out_size, void* d_ws,
                              size_t ws_size, hipStream_t stream) {
  (void)in_sizes; (void)n_in; (void)out_size; (void)ws_size;
  attn_split<<<B_ * HKV * NSPLIT, NTHR, 0, stream>>>((const unsigned char*)d_in[0], (const unsigned char*)d_in[1], (const unsigned char*)d_in[2],
                                                      (const unsigned char*)d_in[3], (const unsigned char*)d_in[4],
                                                      (const unsigned short*)d_in[5], (float*)d_ws);
  attn_combine<<<B_ * HQ, 64, 0, stream>>>((const unsigned short*)d_in[0], (const unsigned short*)d_in[3], (const unsigned short*)d_in[4],
                                           (const unsigned short*)d_in[5], (const float*)d_ws, (unsigned*)d_out);
}
```

```cpp
#include <hip/hip_runtime.h>
#include <hip/hip_bf16.h>
#include <cstdint>

#define NPH 24
#define NSTAT (NPH + 2 + NIL)
#define NIL 0
#define LDSPAD 0

constexpr int B_ = 8, HQ = 32, HKV = 8, CTX = 8192, D = 128, G = HQ / HKV;
constexpr int NGRP = B_ * HKV;
constexpr int NSPLIT = 8;
constexpr int NTILE = CTX / 32;
constexpr int NDYN = NTILE - NSTAT * NSPLIT;
constexpr int GRP_BYTES = CTX * D * 2;
constexpr float SCALE = 0.088388347648318440f;
constexpr float LOG2E = 1.4426950408889634f;
constexpr float NEG_MAX = -3.4028234e38f;
constexpr size_t WS_CTRL = 0;
constexpr size_t WS_PART = 65536;
constexpr int REC_BYTES = 3072;
static_assert(NPH >= 8 && NPH % 4 == 0 && NDYN >= 0 && NDYN + NSPLIT < 65536, "");

#define STAMP(j) do {} while (0)
#define STAMP_FLUSH() do {} while (0)

typedef unsigned u32x4 __attribute__((ext_vector_type(4)));
typedef unsigned u32x2 __attribute__((ext_vector_type(2)));
typedef float f32x4 __attribute__((ext_vector_type(4)));
typedef __bf16 bf16x8 __attribute__((ext_vector_type(8)));
typedef __bf16 bf16x2 __attribute__((ext_vector_type(2)));
typedef float f32x2 __attribute__((ext_vector_type(2)));
typedef unsigned long long u64;

__device__ __forceinline__ f32x4 mfma(u32x4 a, u32x4 b, f32x4 c) {
  return __builtin_amdgcn_mfma_f32_16x16x32_bf16(__builtin_bit_cast(bf16x8, a), __builtin_bit_cast(bf16x8, b), c, 0, 0, 0);
}
template <int CTRL> __device__ __forceinline__ float dpp0(float x) {
  return __builtin_bit_cast(float, __builtin_amdgcn_update_dpp(0, __builtin_bit_cast(int, x), CTRL, 0xf, 0xf, true));
}
template <int CTRL, int BANKS> __device__ __forceinline__ float dpp_into(float old, float x) {
  return __builtin_bit_cast(float, __builtin_amdgcn_update_dpp(__builtin_bit_cast(int, old), __builtin_bit_cast(int, x), CTRL, 0xf, BANKS, false));
}
__device__ __forceinline__ unsigned cvtpk(float lo, float hi) {
  f32x2 v = {lo, hi};
  bf16x2 r = __builtin_convertvector(v, bf16x2);
  return __builtin_bit_cast(unsigned, r);
}
__device__ __forceinline__ float xrow_max(float x) {
  auto s = __builtin_amdgcn_permlane16_swap(__float_as_uint(x), __float_as_uint(x), false, false);
  x = fmaxf(__uint_as_float(s[0]), __uint_as_float(s[1]));
  auto t = __builtin_amdgcn_permlane32_swap(__float_as_uint(x), __float_as_uint(x), false, false);
  return fmaxf(__uint_as_float(t[0]), __uint_as_float(t[1]));
}
__device__ __forceinline__ float xrow_sum(float x) {
  auto s = __builtin_amdgcn_permlane16_swap(__float_as_uint(x), __float_as_uint(x), false, false);
  x = __uint_as_float(s[0]) + __uint_as_float(s[1]);
  auto t = __builtin_amdgcn_permlane32_swap(__float_as_uint(x), __float_as_uint(x), false, false);
  return __uint_as_float(t[0]) + __uint_as_float(t[1]);
}
__device__ __forceinline__ float lo2f(unsigned x) { return __uint_as_float(x << 16); }
__device__ __forceinline__ float hi2f(unsigned x) { return __uint_as_float(x & 0xffff0000u); }

struct State { float m, l; f32x4 acc[8]; };

__device__ __forceinline__ float red4(f32x4 d) { return ((d[0] + dpp0<0x104>(d[1])) + dpp0<0x108>(d[2])) + dpp0<0x10C>(d[3]); }

__device__ __forceinline__ void qk(const u32x4 (&K)[8], u32x4 qf, f32x4 (&d)[8]) {
#pragma unroll
  for (int i = 0; i < 8; ++i) d[i] = mfma(K[i], qf, f32x4{0.f, 0.f, 0.f, 0.f});
}
__device__ __forceinline__ u32x4 softmax_step(const f32x4 (&d)[8], State& st) {
  constexpr float C2 = SCALE * LOG2E;
  float s[8];
#pragma unroll
  for (int i = 0; i < 8; ++i) s[i] = red4(d[i]);
  float mx = fmaxf(fmaxf(fmaxf(s[0], s[1]), fmaxf(s[2], s[3])), fmaxf(fmaxf(s[4], s[5]), fmaxf(s[6], s[7])));
  mx = xrow_max(mx) * C2;
  const float mn = fmaxf(st.m, mx);
  const float alpha = __builtin_amdgcn_exp2f(st.m - mn);
  st.m = mn;
  float p[8];
#pragma unroll
  for (int i = 0; i < 8; ++i) p[i] = __builtin_amdgcn_exp2f(fmaf(s[i], C2, -mn));
  st.l = fmaf(st.l, alpha, ((p[0] + p[1]) + (p[2] + p[3])) + ((p[4] + p[5]) + (p[6] + p[7])));
#pragma unroll
  for (int e = 0; e < 8; ++e) st.acc[e] *= alpha;
  u32x4 pf = {cvtpk(p[0], p[1]), cvtpk(p[2], p[3]), cvtpk(p[4], p[5]), cvtpk(p[6], p[7])};
  return pf;
}
__device__ __forceinline__ void pv(const u32x4 (&V)[8], u32x4 pf, State& st) {
#pragma unroll
  for (int e = 0; e < 8; ++e) {
    const unsigned sel = (e & 1) ? 0x07060302u : 0x05040100u;
    u32x4 vf;
#pragma unroll
    for (int t = 0; t < 4; ++t) vf[t] = __builtin_amdgcn_perm(V[2 * t + 1][e >> 1], V[2 * t][e >> 1], sel);
    st.acc[e] = mfma(vf, pf, st.acc[e]);
  }
}
__device__ __forceinline__ unsigned tile_off(int t) { return (unsigned)(t >> 2) * 32768u + (unsigned)(t & 3) * 1024u; }
#define THR 16
#define STR_(x) #x
#define STR(x) STR_(x)
__device__ __forceinline__ void load_tile(u32x4 (&X)[8], __amdgpu_buffer_rsrc_t r, unsigned voff) {
  if (THR < 63) { asm volatile("s_waitcnt vmcnt(" STR(THR) ")" ::: "memory"); __builtin_amdgcn_sched_barrier(0); }
#pragma unroll
  for (int i = 0; i < 8; ++i) X[i] = __builtin_amdgcn_raw_buffer_load_b128(r, voff, i * 4096, 2);
  asm volatile("" ::: "memory");
  __builtin_amdgcn_sched_barrier(0);
}
__global__ void __launch_bounds__(64, 2)
attn_kernel(const unsigned char* __restrict__ q, const unsigned char* kc, const unsigned char* vc,
            const unsigned char* __restrict__ kt, const unsigned char* __restrict__ vt, const unsigned short* __restrict__ mask,
            unsigned char* ws, unsigned char* out) {
  STAMP(0);
  const int grp = blockIdx.x / NSPLIT, gw = blockIdx.x % NSPLIT;
  const int lane = threadIdx.x, g = lane >> 4, c = lane & 15;
  const __amdgpu_buffer_rsrc_t rk = __builtin_amdgcn_make_buffer_rsrc((void*)(kc + (size_t)grp * GRP_BYTES), 0, GRP_BYTES, 0x00020000);
  const __amdgpu_buffer_rsrc_t rv = __builtin_amdgcn_make_buffer_rsrc((void*)(vc + (size_t)grp * GRP_BYTES), 0, GRP_BYTES, 0x00020000);
  const unsigned kpat = (c >> 2) * 256 + g * 64 + (c & 3) * 16, vpat = lane * 16;
  u64* const ctrl = reinterpret_cast<u64*>(ws + WS_CTRL + (size_t)grp * 256);

  __shared__ __attribute__((aligned(16))) float sc_lds[NPH * 128 + 512];
  u32x4 X0[8], X1[8], X2[8], X3[8];
  const u32x4 qf = *reinterpret_cast<const u32x4*>(q + ((size_t)grp * G + (c & 3)) * (D * 2) + ((c >> 2) + 4 * g) * 16);
  __builtin_amdgcn_sched_barrier(0);
  const unsigned off0 = tile_off(gw);
  load_tile(X0, rk, off0 + kpat);
  load_tile(X1, rk, off0 + 65536 + kpat);
  const u32x4 ktf = *reinterpret_cast<const u32x4*>(kt + (size_t)grp * (D * 2) + ((c & 3) + 4 * g) * 16);
  u32x4 vtf[4];
#pragma unroll
  for (int r = 0; r < 4; ++r) vtf[r] = *reinterpret_cast<const u32x4*>(vt + (size_t)grp * (D * 2) + g * 64 + r * 16);
  const unsigned mk = (reinterpret_cast<const unsigned*>(mask)[grp / (2 * HKV)] >> (((grp / HKV) & 1) * 16)) & 0xffffu;
  const u64 epoch_v = __hip_atomic_load(ctrl, __ATOMIC_RELAXED, __HIP_MEMORY_SCOPE_AGENT);
  const u64 w0 = __hip_atomic_load(ctrl + 16, __ATOMIC_RELAXED, __HIP_MEMORY_SCOPE_AGENT);
  __builtin_amdgcn_sched_barrier(0);
  load_tile(X2, rk, off0 + 2 * 65536 + kpat);
  load_tile(X3, rk, off0 + 3 * 65536 + kpat);
  STAMP(1);

  f32x4 d[8];
  float mx = NEG_MAX;
  {
    float* wr = sc_lds + (c < G ? (g * 4 + c) * 8 : NPH * 128 + lane * 8);
    const int wstep = c < G ? 128 : 0;
#define KSTEP(X, i_) do { \
      qk(X, qf, d); \
      __builtin_amdgcn_sched_barrier(0); \
      load_tile(X, rs, voff + (i_) * 65536); \
      float s_[8]; \
      _Pragma("unroll") for (int i = 0; i < 8; ++i) s_[i] = red4(d[i]); \
      mx = fmaxf(fmaxf(fmaxf(mx, s_[0]), fmaxf(s_[1], s_[2])), fmaxf(fmaxf(fmaxf(s_[3], s_[4]), s_[5]), fmaxf(s_[6], s_[7]))); \
      *reinterpret_cast<f32x4*>(wr) = f32x4{s_[0], s_[1], s_[2], s_[3]}; \
      *reinterpret_cast<f32x4*>(wr + 4) = f32x4{s_[4], s_[5], s_[6], s_[7]}; \
      wr += wstep; \
      __builtin_amdgcn_sched_barrier(0); } while (0)
#pragma unroll 1
    for (int j = 4; j <= NPH; j += 4) {
      const bool more = j < NPH;
      const __amdgpu_buffer_rsrc_t rs = __builtin_amdgcn_make_buffer_rsrc((void*)((more ? kc : vc) + (size_t)grp * GRP_BYTES), 0, GRP_BYTES, 0x00020000);
      const unsigned voff = more ? off0 + j * 65536 + kpat : off0 + vpat;
      KSTEP(X0, 0); KSTEP(X1, 1); KSTEP(X2, 2); KSTEP(X3, 3);
    }
  }

  const u64 epoch = ((u64)(unsigned)__builtin_amdgcn_readfirstlane((unsigned)(epoch_v >> 32)) << 32) | (unsigned)__builtin_amdgcn_readfirstlane((unsigned)epoch_v);
  const u64 tag = epoch + 1;
  u64 claim = w0;
  if (lane == 0 && (w0 >> 16) != (tag & 0xffffffffffffull))
    __hip_atomic_compare_exchange_strong(ctrl + 16, &claim, tag << 16, __ATOMIC_RELAXED, __ATOMIC_RELAXED, __HIP_MEMORY_SCOPE_AGENT);
  const __amdgpu_buffer_rsrc_t rt = __builtin_amdgcn_make_buffer_rsrc((void*)(ctrl + 16), 0, 8, 0x00020000);
  const unsigned toff = lane == 0 ? 0u : 0x40000000u;

  constexpr float C2 = SCALE * LOG2E;
  State st;
  {
    const float stree = red4(mfma(ktf, qf, f32x4{0.f, 0.f, 0.f, 0.f}));
    const float tt = (stree * SCALE + (1.0f - lo2f(mk)) * -1.0e9f) * LOG2E;
    const bool seed = (gw == 0);
    mx = xrow_max(mx) * C2;
    st.m = fmaxf(seed ? fmaxf(tt, mx) : mx, NEG_MAX);
    const float pt = seed ? __builtin_amdgcn_exp2f(tt - st.m) : 0.f;
    st.l = g == 0 ? pt : 0.f;
#pragma unroll
    for (int e = 0; e < 8; ++e)
#pragma unroll
      for (int r = 0; r < 4; ++r) { const float x = (e & 1) ? hi2f(vtf[r][e >> 1]) : lo2f(vtf[r][e >> 1]); st.acc[e][r] = seed ? pt * x : 0.f; }
  }
  __builtin_amdgcn_sched_barrier(0);

  int tk = 0;
  {
    const float* rd = sc_lds + (g * 4 + (c & 3)) * 8;
#define VSTEP(X, rs_, voff_, TK) do { \
      const f32x4 s0 = *reinterpret_cast<const f32x4*>(rd), s1 = *reinterpret_cast<const f32x4*>(rd + 4); \
      rd += 128; \
      float p_[8]; \
      _Pragma("unroll") for (int i = 0; i < 4; ++i) { p_[i] = __builtin_amdgcn_exp2f(fmaf(s0[i], C2, -st.m)); p_[4 + i] = __builtin_amdgcn_exp2f(fmaf(s1[i], C2, -st.m)); } \
      st.l += ((p_[0] + p_[1]) + (p_[2] + p_[3])) + ((p_[4] + p_[5]) + (p_[6] + p_[7])); \
      asm volatile("" : "+v"(st.l)); \
      const u32x4 pf = {cvtpk(p_[0], p_[1]), cvtpk(p_[2], p_[3]), cvtpk(p_[4], p_[5]), cvtpk(p_[6], p_[7])}; \
      pv(X, pf, st); \
      __builtin_amdgcn_sched_barrier(0); \
      if (TK) { tk = __builtin_amdgcn_raw_ptr_buffer_atomic_add_i32(1, rt, toff | ((!more && NIL == 0) ? 0u : 0x40000000u), 0, 0); __builtin_amdgcn_sched_barrier(0); } \
      load_tile(X, rs_, voff_); } while (0)
#pragma unroll 1
    for (int j = 4; j <= NPH; j += 4) {
      const bool more = j < NPH;
      const __amdgpu_buffer_rsrc_t rs = __builtin_amdgcn_make_buffer_rsrc((void*)((more ? vc : kc) + (size_t)grp * GRP_BYTES), 0, GRP_BYTES, 0x00020000);
      const unsigned vo = off0 + j * 65536 + vpat, ko = off0 + j * 65536 + (more ? vpat : kpat);
      const unsigned st1 = more ? 65536 : 0, st2 = more ? 2 * 65536 : 65536, st3 = more ? 3 * 65536 : 65536;
      VSTEP(X0, rs, ko, 0); VSTEP(X1, rv, vo + st1, 0); VSTEP(X2, rs, ko + st2, 1); VSTEP(X3, rv, vo + st3, 0);
      asm volatile("" :: "v"(claim));
    }
  }
  STAMP(3);

  int ta, tb = 0;
  int n = NPH + 2;
  int last = 0;
#define NEXT(tx) do { \
    if (n < NSTAT) last = gw + n * NSPLIT; \
    else if (last < NTILE) last = NSTAT * NSPLIT + (__builtin_amdgcn_readfirstlane(tk) & 0xffff); \
    tx = last; ++n; \
    tk = __builtin_amdgcn_raw_ptr_buffer_atomic_add_i32(1, rt, toff | ((n >= NSTAT && last < NTILE) ? 0u : 0x40000000u), 0, 0); \
    __builtin_amdgcn_sched_barrier(0); } while (0)
#pragma unroll 1
  do {
    qk(X0, qf, d);
    __builtin_amdgcn_sched_barrier(0);
    NEXT(ta);
    unsigned off = tile_off(ta);
    load_tile(X0, rk, off + kpat);
    { u32x4 pf = softmax_step(d, st); pv(X1, pf, st); }
    __builtin_amdgcn_sched_barrier(0);
    load_tile(X1, rv, off + vpat);
    const bool bv = tb < NTILE;
    if (bv) qk(X2, qf, d);
    __builtin_amdgcn_sched_barrier(0);
    NEXT(tb);
    off = tile_off(tb);
    load_tile(X2, rk, off + kpat);
    if (bv) { u32x4 pf = softmax_step(d, st); pv(X3, pf, st); }
    __builtin_amdgcn_sched_barrier(0);
    load_tile(X3, rv, off + vpat);
  } while (ta < NTILE);
  STAMP(2);

  float o[8], pm, pl;
#pragma unroll
  for (int e = 0; e < 8; ++e)
    o[e] = dpp_into<0x11C, 8>(dpp_into<0x118, 4>(dpp_into<0x114, 2>(st.acc[e][0], st.acc[e][1]), st.acc[e][2]), st.acc[e][3]);
  {
    const float ls = xrow_sum(st.l);
    pm = dpp_into<0x11C, 8>(dpp_into<0x118, 4>(dpp_into<0x114, 2>(st.m, st.m), st.m), st.m);
    pl = dpp_into<0x11C, 8>(dpp_into<0x118, 4>(dpp_into<0x114, 2>(ls, ls), ls), ls);
  }
  const unsigned tag0 = (unsigned)tag ^ 0x9e3779b9u, tag1 = (unsigned)(tag >> 32) ^ 0x5bd1e995u;
  const __amdgpu_buffer_rsrc_t rp = __builtin_amdgcn_make_buffer_rsrc((void*)(ws + WS_PART + (size_t)grp * NSPLIT * REC_BYTES), 0, NSPLIT * REC_BYTES, 0x00020000);
  if (ta != NTILE + NSPLIT - 1) {
    const u32x4 w0v = {__float_as_uint(o[0]), __float_as_uint(o[1]), __float_as_uint(o[2]), __float_as_uint(o[3])};
    const u32x4 w1v = {__float_as_uint(o[4]), __float_as_uint(o[5]), __float_as_uint(o[6]), __float_as_uint(o[7])};
    const u32x4 w2v = {__float_as_uint(pm), __float_as_uint(pl), tag0 ^ w0v[0] ^ w0v[2] ^ w1v[0] ^ w1v[2] ^ __float_as_uint(pm),
                       tag1 ^ w0v[1] ^ w0v[3] ^ w1v[1] ^ w1v[3] ^ __float_as_uint(pl)};
    __builtin_amdgcn_raw_buffer_store_b128(w0v, rp, vpat, gw * REC_BYTES, 16);
    __builtin_amdgcn_raw_buffer_store_b128(w1v, rp, vpat + 1024, gw * REC_BYTES, 16);
    __builtin_amdgcn_raw_buffer_store_b128(w2v, rp, vpat + 2048, gw * REC_BYTES, 16);
    STAMP_FLUSH();
    return;
  }
  u32x4 R[NSPLIT - 1][3];
#pragma unroll 1
  for (int spin = 0; spin < (1 << 18); ++spin) {
#pragma unroll
    for (int j = 0; j < NSPLIT - 1; ++j) {
      const int s = (gw + 1 + j) & (NSPLIT - 1);
#pragma unroll
      for (int k = 0; k < 3; ++k) R[j][k] = __builtin_amdgcn_raw_buffer_load_b128(rp, vpat + k * 1024, s * REC_BYTES, 16);
    }
    asm volatile("" ::: "memory");
    unsigned bad = 0;
#pragma unroll
    for (int j = 0; j < NSPLIT - 1; ++j) {
      bad |= tag0 ^ R[j][0][0] ^ R[j][0][2] ^ R[j][1][0] ^ R[j][1][2] ^ R[j][2][0] ^ R[j][2][2];
      bad |= tag1 ^ R[j][0][1] ^ R[j][0][3] ^ R[j][1][1] ^ R[j][1][3] ^ R[j][2][1] ^ R[j][2][3];
    }
    if (__builtin_amdgcn_ballot_w64(bad != 0) == 0) break;
  }
  STAMP(4);
  float M = pm;
#pragma unroll
  for (int j = 0; j < NSPLIT - 1; ++j) M = fmaxf(M, __uint_as_float(R[j][2][0]));
  {
    const float w = __builtin_amdgcn_exp2f(pm - M);
    pl *= w;
#pragma unroll
    for (int e = 0; e < 8; ++e) o[e] *= w;
  }
#pragma unroll
  for (int j = 0; j < NSPLIT - 1; ++j) {
    const float w = __builtin_amdgcn_exp2f(__uint_as_float(R[j][2][0]) - M);
    pl = fmaf(w, __uint_as_float(R[j][2][1]), pl);
#pragma unroll
    for (int e = 0; e < 8; ++e) o[e] = fmaf(w, __uint_as_float(R[j][e >> 2][e & 3]), o[e]);
  }
  const float inv = 1.0f / pl;
  const u32x4 res = {cvtpk(o[0] * inv, o[1] * inv), cvtpk(o[2] * inv, o[3] * inv), cvtpk(o[4] * inv, o[5] * inv), cvtpk(o[6] * inv, o[7] * inv)};
  *reinterpret_cast<u32x4*>(out + ((size_t)grp * G + (c & 3)) * (D * 2) + (4 * g + (c >> 2)) * 16) = res;
  if (lane == 0) __hip_atomic_store(ctrl, tag, __ATOMIC_RELAXED, __HIP_MEMORY_SCOPE_AGENT);
  STAMP(5);
  STAMP_FLUSH();
}

extern "C" void kernel_launch(void* const* d_in, const int* in_sizes, int n_in, void* d_out, int out_size, void* d_ws,
                              size_t ws_size, hipStream_t stream) {
  (void)in_sizes; (void)n_in; (void)out_size; (void)ws_size;
  attn_kernel<<<NGRP * NSPLIT, 64, LDSPAD, stream>>>((const unsigned char*)d_in[0], (const unsigned char*)d_in[1], (const unsigned char*)d_in[2],
                                                     (const unsigned char*)d_in[3], (const unsigned char*)d_in[4],
                                                     (const unsigned short*)d_in[5], (unsigned char*)d_ws, (unsigned char*)d_out);
}
```

```cpp
#define DIAG 1
#include <hip/hip_runtime.h>
#include <hip/hip_bf16.h>
#include <cstdint>

#define NPH 20
#define NSTAT (NPH + 2 + NIL)
#define NIL 0
#define LDSPAD 0

constexpr int B_ = 8, HQ = 32, HKV = 8, CTX = 8192, D = 128, G = HQ / HKV;
constexpr int NGRP = B_ * HKV;
constexpr int NSPLIT = 8;
constexpr int NTILE = CTX / 32;
constexpr int NDYN = NTILE - NSTAT * NSPLIT;
constexpr int GRP_BYTES = CTX * D * 2;
constexpr float SCALE = 0.088388347648318440f;
constexpr float LOG2E = 1.4426950408889634f;
constexpr float NEG_MAX = -3.4028234e38f;
constexpr size_t WS_CTRL = 0;
constexpr size_t WS_PART = 65536;
constexpr int REC_BYTES = 3072;
static_assert(NPH >= 8 && NPH % 4 == 0 && NDYN >= 0 && NDYN + NSPLIT < 65536, "");

#define STAMP(j) do {} while (0)
#define STAMP_FLUSH() do {} while (0)

typedef unsigned u32x4 __attribute__((ext_vector_type(4)));
typedef unsigned u32x2 __attribute__((ext_vector_type(2)));
typedef float f32x4 __attribute__((ext_vector_type(4)));
typedef __bf16 bf16x8 __attribute__((ext_vector_type(8)));
typedef __bf16 bf16x2 __attribute__((ext_vector_type(2)));
typedef float f32x2 __attribute__((ext_vector_type(2)));
typedef unsigned long long u64;

__device__ __forceinline__ f32x4 mfma(u32x4 a, u32x4 b, f32x4 c) {
  return __builtin_amdgcn_mfma_f32_16x16x32_bf16(__builtin_bit_cast(bf16x8, a), __builtin_bit_cast(bf16x8, b), c, 0, 0, 0);
}
template <int CTRL> __device__ __forceinline__ float dpp0(float x) {
  return __builtin_bit_cast(float, __builtin_amdgcn_update_dpp(0, __builtin_bit_cast(int, x), CTRL, 0xf, 0xf, true));
}
template <int CTRL, int BANKS> __device__ __forceinline__ float dpp_into(float old, float x) {
  return __builtin_bit_cast(float, __builtin_amdgcn_update_dpp(__builtin_bit_cast(int, old), __builtin_bit_cast(int, x), CTRL, 0xf, BANKS, false));
}
__device__ __forceinline__ unsigned cvtpk(float lo, float hi) {
  f32x2 v = {lo, hi};
  bf16x2 r = __builtin_convertvector(v, bf16x2);
  return __builtin_bit_cast(unsigned, r);
}
__device__ __forceinline__ float xrow_max(float x) {
  auto s = __builtin_amdgcn_permlane16_swap(__float_as_uint(x), __float_as_uint(x), false, false);
  x = fmaxf(__uint_as_float(s[0]), __uint_as_float(s[1]));
  auto t = __builtin_amdgcn_permlane32_swap(__float_as_uint(x), __float_as_uint(x), false, false);
  return fmaxf(__uint_as_float(t[0]), __uint_as_float(t[1]));
}
__device__ __forceinline__ float xrow_sum(float x) {
  auto s = __builtin_amdgcn_permlane16_swap(__float_as_uint(x), __float_as_uint(x), false, false);
  x = __uint_as_float(s[0]) + __uint_as_float(s[1]);
  auto t = __builtin_amdgcn_permlane32_swap(__float_as_uint(x), __float_as_uint(x), false, false);
  return __uint_as_float(t[0]) + __uint_as_float(t[1]);
}
__device__ __forceinline__ float lo2f(unsigned x) { return __uint_as_float(x << 16); }
__device__ __forceinline__ float hi2f(unsigned x) { return __uint_as_float(x & 0xffff0000u); }

struct State { float m, l; f32x4 acc[8]; };

__device__ __forceinline__ float red4(f32x4 d) { return ((d[0] + dpp0<0x104>(d[1])) + dpp0<0x108>(d[2])) + dpp0<0x10C>(d[3]); }

__device__ __forceinline__ void qk(const u32x4 (&K)[8], u32x4 qf, f32x4 (&d)[8]) {
#pragma unroll
  for (int i = 0; i < 8; ++i) d[i] = mfma(K[i], qf, f32x4{0.f, 0.f, 0.f, 0.f});
}
__device__ __forceinline__ u32x4 softmax_step(const f32x4 (&d)[8], State& st) {
  constexpr float C2 = SCALE * LOG2E;
  float s[8];
#pragma unroll
  for (int i = 0; i < 8; ++i) s[i] = red4(d[i]);
  float mx = fmaxf(fmaxf(fmaxf(s[0], s[1]), fmaxf(s[2], s[3])), fmaxf(fmaxf(s[4], s[5]), fmaxf(s[6], s[7])));
  mx = xrow_max(mx) * C2;
  const float mn = fmaxf(st.m, mx);
  const float alpha = __builtin_amdgcn_exp2f(st.m - mn);
  st.m = mn;
  float p[8];
#pragma unroll
  for (int i = 0; i < 8; ++i) p[i] = __builtin_amdgcn_exp2f(fmaf(s[i], C2, -mn));
  st.l = fmaf(st.l, alpha, ((p[0] + p[1]) + (p[2] + p[3])) + ((p[4] + p[5]) + (p[6] + p[7])));
#pragma unroll
  for (int e = 0; e < 8; ++e) st.acc[e] *= alpha;
  u32x4 pf = {cvtpk(p[0], p[1]), cvtpk(p[2], p[3]), cvtpk(p[4], p[5]), cvtpk(p[6], p[7])};
  return pf;
}
__device__ __forceinline__ void pv(const u32x4 (&V)[8], u32x4 pf, State& st) {
#pragma unroll
  for (int e = 0; e < 8; ++e) {
    const unsigned sel = (e & 1) ? 0x07060302u : 0x05040100u;
    u32x4 vf;
#pragma unroll
    for (int t = 0; t < 4; ++t) vf[t] = __builtin_amdgcn_perm(V[2 * t + 1][e >> 1], V[2 * t][e >> 1], sel);
    st.acc[e] = mfma(vf, pf, st.acc[e]);
  }
}
__device__ __forceinline__ unsigned tile_off(int t) { return (unsigned)(t >> 2) * 32768u + (unsigned)(t & 3) * 1024u; }
#define THR 63
#define STR_(x) #x
#define STR(x) STR_(x)
__device__ __forceinline__ void load_tile(u32x4 (&X)[8], __amdgpu_buffer_rsrc_t r, unsigned voff) {
  if (THR < 63) { asm volatile("s_waitcnt vmcnt(" STR(THR) ")" ::: "memory"); __builtin_amdgcn_sched_barrier(0); }
#pragma unroll
  for (int i = 0; i < 8; ++i) X[i] = __builtin_amdgcn_raw_buffer_load_b128(r, voff, i * 4096, 19);
  asm volatile("" ::: "memory");
  __builtin_amdgcn_sched_barrier(0);
}
__global__ void __launch_bounds__(64, 2)
attn_kernel(const unsigned char* __restrict__ q, const unsigned char* kc, const unsigned char* vc,
            const unsigned char* __restrict__ kt, const unsigned char* __restrict__ vt, const unsigned short* __restrict__ mask,
            unsigned char* ws, unsigned char* out) {
  STAMP(0);
  const unsigned t_start = (unsigned)__builtin_amdgcn_s_memrealtime();
  const int grp = blockIdx.x / NSPLIT, gw = blockIdx.x % NSPLIT;
  const int lane = threadIdx.x, g = lane >> 4, c = lane & 15;
  const __amdgpu_buffer_rsrc_t rk = __builtin_amdgcn_make_buffer_rsrc((void*)(kc + (size_t)grp * GRP_BYTES), 0, GRP_BYTES, 0x00020000);
  const __amdgpu_buffer_rsrc_t rv = __builtin_amdgcn_make_buffer_rsrc((void*)(vc + (size_t)grp * GRP_BYTES), 0, GRP_BYTES, 0x00020000);
  const unsigned kpat = (c >> 2) * 256 + g * 64 + (c & 3) * 16, vpat = lane * 16;
  u64* const ctrl = reinterpret_cast<u64*>(ws + WS_CTRL + (size_t)grp * 256);

  __shared__ __attribute__((aligned(16))) float sc_lds[NPH * 128 + 512];
  u32x4 X0[8], X1[8], X2[8], X3[8];
  const u32x4 qf = *reinterpret_cast<const u32x4*>(q + ((size_t)grp * G + (c & 3)) * (D * 2) + ((c >> 2) + 4 * g) * 16);
  __builtin_amdgcn_sched_barrier(0);
  const unsigned off0 = tile_off(gw);
  load_tile(X0, rk, off0 + kpat);
  load_tile(X1, rk, off0 + 65536 + kpat);
  const u32x4 ktf = *reinterpret_cast<const u32x4*>(kt + (size_t)grp * (D * 2) + ((c & 3) + 4 * g) * 16);
  u32x4 vtf[4];
#pragma unroll
  for (int r = 0; r < 4; ++r) vtf[r] = *reinterpret_cast<const u32x4*>(vt + (size_t)grp * (D * 2) + g * 64 + r * 16);
  const unsigned mk = (reinterpret_cast<const unsigned*>(mask)[grp / (2 * HKV)] >> (((grp / HKV) & 1) * 16)) & 0xffffu;
  const u64 epoch_v = __hip_atomic_load(ctrl, __ATOMIC_RELAXED, __HIP_MEMORY_SCOPE_AGENT);
  const u64 w0 = __hip_atomic_load(ctrl + 16, __ATOMIC_RELAXED, __HIP_MEMORY_SCOPE_AGENT);
  __builtin_amdgcn_sched_barrier(0);
  load_tile(X2, rk, off0 + 2 * 65536 + kpat);
  load_tile(X3, rk, off0 + 3 * 65536 + kpat);
  STAMP(1);

  f32x4 d[8];
  float mx = NEG_MAX;
  {
    float* wr = sc_lds + (c < G ? (g * 4 + c) * 8 : NPH * 128 + lane * 8);
    const int wstep = c < G ? 128 : 0;
#define KSTEP(X, i_) do { \
      qk(X, qf, d); \
      __builtin_amdgcn_sched_barrier(0); \
      load_tile(X, rs, voff + (i_) * 65536); \
      float s_[8]; \
      _Pragma("unroll") for (int i = 0; i < 8; ++i) s_[i] = red4(d[i]); \
      mx = fmaxf(fmaxf(fmaxf(mx, s_[0]), fmaxf(s_[1], s_[2])), fmaxf(fmaxf(fmaxf(s_[3], s_[4]), s_[5]), fmaxf(s_[6], s_[7]))); \
      *reinterpret_cast<f32x4*>(wr) = f32x4{s_[0], s_[1], s_[2], s_[3]}; \
      *reinterpret_cast<f32x4*>(wr + 4) = f32x4{s_[4], s_[5], s_[6], s_[7]}; \
      wr += wstep; \
      __builtin_amdgcn_sched_barrier(0); } while (0)
#pragma unroll 1
    for (int j = 4; j <= NPH; j += 4) {
      const bool more = j < NPH;
      const __amdgpu_buffer_rsrc_t rs = __builtin_amdgcn_make_buffer_rsrc((void*)((more ? kc : vc) + (size_t)grp * GRP_BYTES), 0, GRP_BYTES, 0x00020000);
      const unsigned voff = more ? off0 + j * 65536 + kpat : off0 + vpat;
      KSTEP(X0, 0); KSTEP(X1, 1); KSTEP(X2, 2); KSTEP(X3, 3);
    }
  }

  unsigned* const dg = reinterpret_cast<unsigned*>(ws + (8 << 20)) + grp * 64;
  const unsigned t_k = (unsigned)__builtin_amdgcn_s_memrealtime();
  if (lane == 0) { __hip_atomic_store(dg + gw, t_start, __ATOMIC_RELAXED, __HIP_MEMORY_SCOPE_AGENT); __hip_atomic_store(dg + 8 + gw, t_k, __ATOMIC_RELAXED, __HIP_MEMORY_SCOPE_AGENT); }
  const u64 epoch = ((u64)(unsigned)__builtin_amdgcn_readfirstlane((unsigned)(epoch_v >> 32)) << 32) | (unsigned)__builtin_amdgcn_readfirstlane((unsigned)epoch_v);
  const u64 tag = epoch + 1;
  u64 claim = w0;
  if (lane == 0 && (w0 >> 16) != (tag & 0xffffffffffffull))
    __hip_atomic_compare_exchange_strong(ctrl + 16, &claim, tag << 16, __ATOMIC_RELAXED, __ATOMIC_RELAXED, __HIP_MEMORY_SCOPE_AGENT);
  const __amdgpu_buffer_rsrc_t rt = __builtin_amdgcn_make_buffer_rsrc((void*)(ctrl + 16), 0, 8, 0x00020000);
  const unsigned toff = lane == 0 ? 0u : 0x40000000u;

  constexpr float C2 = SCALE * LOG2E;
  State st;
  {
    const float stree = red4(mfma(ktf, qf, f32x4{0.f, 0.f, 0.f, 0.f}));
    const float tt = (stree * SCALE + (1.0f - lo2f(mk)) * -1.0e9f) * LOG2E;
    const bool seed = (gw == 0);
    mx = xrow_max(mx) * C2;
    st.m = fmaxf(seed ? fmaxf(tt, mx) : mx, NEG_MAX);
    const float pt = seed ? __builtin_amdgcn_exp2f(tt - st.m) : 0.f;
    st.l = g == 0 ? pt : 0.f;
#pragma unroll
    for (int e = 0; e < 8; ++e)
#pragma unroll
      for (int r = 0; r < 4; ++r) { const float x = (e & 1) ? hi2f(vtf[r][e >> 1]) : lo2f(vtf[r][e >> 1]); st.acc[e][r] = seed ? pt * x : 0.f; }
  }
  __builtin_amdgcn_sched_barrier(0);

  int tk = 0;
  {
    const float* rd = sc_lds + (g * 4 + (c & 3)) * 8;
#define VSTEP(X, rs_, voff_, TK) do { \
      const f32x4 s0 = *reinterpret_cast<const f32x4*>(rd), s1 = *reinterpret_cast<const f32x4*>(rd + 4); \
      rd += 128; \
      float p_[8]; \
      _Pragma("unroll") for (int i = 0; i < 4; ++i) { p_[i] = __builtin_amdgcn_exp2f(fmaf(s0[i], C2, -st.m)); p_[4 + i] = __builtin_amdgcn_exp2f(fmaf(s1[i], C2, -st.m)); } \
      st.l += ((p_[0] + p_[1]) + (p_[2] + p_[3])) + ((p_[4] + p_[5]) + (p_[6] + p_[7])); \
      asm volatile("" : "+v"(st.l)); \
      const u32x4 pf = {cvtpk(p_[0], p_[1]), cvtpk(p_[2], p_[3]), cvtpk(p_[4], p_[5]), cvtpk(p_[6], p_[7])}; \
      pv(X, pf, st); \
      __builtin_amdgcn_sched_barrier(0); \
      if (TK) { tk = __builtin_amdgcn_raw_ptr_buffer_atomic_add_i32(1, rt, toff | ((!more && NIL == 0) ? 0u : 0x40000000u), 0, 0); __builtin_amdgcn_sched_barrier(0); } \
      load_tile(X, rs_, voff_); } while (0)
#pragma unroll 1
    for (int j = 4; j <= NPH; j += 4) {
      const bool more = j < NPH;
      const __amdgpu_buffer_rsrc_t rs = __builtin_amdgcn_make_buffer_rsrc((void*)((more ? vc : kc) + (size_t)grp * GRP_BYTES), 0, GRP_BYTES, 0x00020000);
      const unsigned vo = off0 + j * 65536 + vpat, ko = off0 + j * 65536 + (more ? vpat : kpat);
      const unsigned st1 = more ? 65536 : 0, st2 = more ? 2 * 65536 : 65536, st3 = more ? 3 * 65536 : 65536;
      VSTEP(X0, rs, ko, 0); VSTEP(X1, rv, vo + st1, 0); VSTEP(X2, rs, ko + st2, 1); VSTEP(X3, rv, vo + st3, 0);
      asm volatile("" :: "v"(claim));
    }
  }
  STAMP(3);

  int ta, tb = 0;
  int n = NPH + 2;
  int last = 0;
#define NEXT(tx) do { \
    if (n < NSTAT) last = gw + n * NSPLIT; \
    else if (last < NTILE) last = NSTAT * NSPLIT + (__builtin_amdgcn_readfirstlane(tk) & 0xffff); \
    tx = last; ++n; \
    tk = __builtin_amdgcn_raw_ptr_buffer_atomic_add_i32(1, rt, toff | ((n >= NSTAT && last < NTILE) ? 0u : 0x40000000u), 0, 0); \
    __builtin_amdgcn_sched_barrier(0); } while (0)
#pragma unroll 1
  do {
    qk(X0, qf, d);
    __builtin_amdgcn_sched_barrier(0);
    NEXT(ta);
    unsigned off = tile_off(ta);
    load_tile(X0, rk, off + kpat);
    { u32x4 pf = softmax_step(d, st); pv(X1, pf, st); }
    __builtin_amdgcn_sched_barrier(0);
    load_tile(X1, rv, off + vpat);
    const bool bv = tb < NTILE;
    if (bv) qk(X2, qf, d);
    __builtin_amdgcn_sched_barrier(0);
    NEXT(tb);
    off = tile_off(tb);
    load_tile(X2, rk, off + kpat);
    if (bv) { u32x4 pf = softmax_step(d, st); pv(X3, pf, st); }
    __builtin_amdgcn_sched_barrier(0);
    load_tile(X3, rv, off + vpat);
  } while (ta < NTILE);
  STAMP(2);

  float o[8], pm, pl;
#pragma unroll
  for (int e = 0; e < 8; ++e)
    o[e] = dpp_into<0x11C, 8>(dpp_into<0x118, 4>(dpp_into<0x114, 2>(st.acc[e][0], st.acc[e][1]), st.acc[e][2]), st.acc[e][3]);
  {
    const float ls = xrow_sum(st.l);
    pm = dpp_into<0x11C, 8>(dpp_into<0x118, 4>(dpp_into<0x114, 2>(st.m, st.m), st.m), st.m);
    pl = dpp_into<0x11C, 8>(dpp_into<0x118, 4>(dpp_into<0x114, 2>(ls, ls), ls), ls);
  }
  const unsigned tag0 = (unsigned)tag ^ 0x9e3779b9u, tag1 = (unsigned)(tag >> 32) ^ 0x5bd1e995u;
  const __amdgpu_buffer_rsrc_t rp = __builtin_amdgcn_make_buffer_rsrc((void*)(ws + WS_PART + (size_t)grp * NSPLIT * REC_BYTES), 0, NSPLIT * REC_BYTES, 0x00020000);
  if (ta != NTILE + NSPLIT - 1) {
    const u32x4 w0v = {__float_as_uint(o[0]), __float_as_uint(o[1]), __float_as_uint(o[2]), __float_as_uint(o[3])};
    const u32x4 w1v = {__float_as_uint(o[4]), __float_as_uint(o[5]), __float_as_uint(o[6]), __float_as_uint(o[7])};
    const u32x4 w2v = {__float_as_uint(pm), __float_as_uint(pl), tag0 ^ w0v[0] ^ w0v[2] ^ w1v[0] ^ w1v[2] ^ __float_as_uint(pm),
                       tag1 ^ w0v[1] ^ w0v[3] ^ w1v[1] ^ w1v[3] ^ __float_as_uint(pl)};
    __builtin_amdgcn_raw_buffer_store_b128(w0v, rp, vpat, gw * REC_BYTES, 16);
    __builtin_amdgcn_raw_buffer_store_b128(w1v, rp, vpat + 1024, gw * REC_BYTES, 16);
    __builtin_amdgcn_raw_buffer_store_b128(w2v, rp, vpat + 2048, gw * REC_BYTES, 16);
    if (DIAG == 2) while ((unsigned)__builtin_amdgcn_s_memrealtime() - t_start < 10000u) __builtin_amdgcn_s_sleep(1);
    return;
  }
  u32x4 R[NSPLIT - 1][3];
#pragma unroll 1
  for (int spin = 0; spin < (1 << 18); ++spin) {
#pragma unroll
    for (int j = 0; j < NSPLIT - 1; ++j) {
      const int s = (gw + 1 + j) & (NSPLIT - 1);
#pragma unroll
      for (int k = 0; k < 3; ++k) R[j][k] = __builtin_amdgcn_raw_buffer_load_b128(rp, vpat + k * 1024, s * REC_BYTES, 16);
    }
    asm volatile("" ::: "memory");
    unsigned bad = 0;
#pragma unroll
    for (int j = 0; j < NSPLIT - 1; ++j) {
      bad |= tag0 ^ R[j][0][0] ^ R[j][0][2] ^ R[j][1][0] ^ R[j][1][2] ^ R[j][2][0] ^ R[j][2][2];
      bad |= tag1 ^ R[j][0][1] ^ R[j][0][3] ^ R[j][1][1] ^ R[j][1][3] ^ R[j][2][1] ^ R[j][2][3];
    }
    if (__builtin_amdgcn_ballot_w64(bad != 0) == 0) break;
  }
  STAMP(4);
  float M = pm;
#pragma unroll
  for (int j = 0; j < NSPLIT - 1; ++j) M = fmaxf(M, __uint_as_float(R[j][2][0]));
  {
    const float w = __builtin_amdgcn_exp2f(pm - M);
    pl *= w;
#pragma unroll
    for (int e = 0; e < 8; ++e) o[e] *= w;
  }
#pragma unroll
  for (int j = 0; j < NSPLIT - 1; ++j) {
    const float w = __builtin_amdgcn_exp2f(__uint_as_float(R[j][2][0]) - M);
    pl = fmaf(w, __uint_as_float(R[j][2][1]), pl);
#pragma unroll
    for (int e = 0; e < 8; ++e) o[e] = fmaf(w, __uint_as_float(R[j][e >> 2][e & 3]), o[e]);
  }
  const float inv = 1.0f / pl;
  const u32x4 res = {cvtpk(o[0] * inv, o[1] * inv), cvtpk(o[2] * inv, o[3] * inv), cvtpk(o[4] * inv, o[5] * inv), cvtpk(o[6] * inv, o[7] * inv)};
  { const __amdgpu_buffer_rsrc_t ro = __builtin_amdgcn_make_buffer_rsrc((void*)(out + (size_t)grp * G * D * 2), 0, G * D * 2, 0x00020000);
    __builtin_amdgcn_raw_buffer_store_b128(res, ro, (c & 3) * (D * 2) + (4 * g + (c >> 2)) * 16, 0, 17); }
  if (lane == 0) __hip_atomic_store(ctrl, tag, __ATOMIC_RELAXED, __HIP_MEMORY_SCOPE_AGENT);
  {
    const unsigned t_end = (unsigned)__builtin_amdgcn_s_memrealtime();
    unsigned back = 0, kmax = 0;
    for (int j = 0; j < 8; ++j) { const unsigned o_ = __hip_atomic_load(dg + j, __ATOMIC_RELAXED, __HIP_MEMORY_SCOPE_AGENT); const int df = (int)(t_start - o_); if (df > (int)back && df < 5000) back = df; }
    const unsigned t0 = t_start - back;
    for (int j = 0; j < 8; ++j) { const unsigned o_ = __hip_atomic_load(dg + 8 + j, __ATOMIC_RELAXED, __HIP_MEMORY_SCOPE_AGENT) - t0; if (o_ < 6000u && o_ > kmax) kmax = o_; }
    unsigned dl = 10000u;
    if (DIAG == 3) dl += (t_end - t0);
    if (DIAG == 4) dl += kmax;
    const unsigned ref = DIAG == 2 ? t_start : t0;
    while ((unsigned)__builtin_amdgcn_s_memrealtime() - ref < dl) __builtin_amdgcn_s_sleep(1);
  }
  STAMP(5);
  STAMP_FLUSH();
}

extern "C" void kernel_launch(void* const* d_in, const int* in_sizes, int n_in, void* d_out, int out_size, void* d_ws,
                              size_t ws_size, hipStream_t stream) {
  (void)in_sizes; (void)n_in; (void)out_size; (void)ws_size;
  attn_kernel<<<NGRP * NSPLIT, 64, LDSPAD, stream>>>((const unsigned char*)d_in[0], (const unsigned char*)d_in[1], (const unsigned char*)d_in[2],
                                                     (const unsigned char*)d_in[3], (const unsigned char*)d_in[4],
                                                     (const unsigned short*)d_in[5], (unsigned char*)d_ws, (unsigned char*)d_out);
}
```

```cpp
#define AUXK 2
#define AUXV 2
#include <hip/hip_runtime.h>
#include <hip/hip_bf16.h>
#include <cstdint>

#define NPH 20
#define NSTAT (NPH + 2 + NIL)
#define NIL 0
#define LDSPAD 0

constexpr int B_ = 8, HQ = 32, HKV = 8, CTX = 8192, D = 128, G = HQ / HKV;
constexpr int NGRP = B_ * HKV;
constexpr int NSPLIT = 8;
constexpr int NTILE = CTX / 32;
constexpr int NDYN = NTILE - NSTAT * NSPLIT;
constexpr int GRP_BYTES = CTX * D * 2;
constexpr float SCALE = 0.088388347648318440f;
constexpr float LOG2E = 1.4426950408889634f;
constexpr float NEG_MAX = -3.4028234e38f;
constexpr size_t WS_CTRL = 0;
constexpr size_t WS_PART = 65536;
constexpr int REC_BYTES = 3072;
static_assert(NPH >= 8 && NPH % 4 == 0 && NDYN >= 0 && NDYN + NSPLIT < 65536, "");

#define STAMP(j) do {} while (0)
#define STAMP_FLUSH() do {} while (0)

typedef unsigned u32x4 __attribute__((ext_vector_type(4)));
typedef unsigned u32x2 __attribute__((ext_vector_type(2)));
typedef float f32x4 __attribute__((ext_vector_type(4)));
typedef __bf16 bf16x8 __attribute__((ext_vector_type(8)));
typedef __bf16 bf16x2 __attribute__((ext_vector_type(2)));
typedef float f32x2 __attribute__((ext_vector_type(2)));
typedef unsigned long long u64;

__device__ __forceinline__ f32x4 mfma(u32x4 a, u32x4 b, f32x4 c) {
  return __builtin_amdgcn_mfma_f32_16x16x32_bf16(__builtin_bit_cast(bf16x8, a), __builtin_bit_cast(bf16x8, b), c, 0, 0, 0);
}
template <int CTRL> __device__ __forceinline__ float dpp0(float x) {
  return __builtin_bit_cast(float, __builtin_amdgcn_update_dpp(0, __builtin_bit_cast(int, x), CTRL, 0xf, 0xf, true));
}
template <int CTRL, int BANKS> __device__ __forceinline__ float dpp_into(float old, float x) {
  return __builtin_bit_cast(float, __builtin_amdgcn_update_dpp(__builtin_bit_cast(int, old), __builtin_bit_cast(int, x), CTRL, 0xf, BANKS, false));
}
__device__ __forceinline__ unsigned cvtpk(float lo, float hi) {
  f32x2 v = {lo, hi};
  bf16x2 r = __builtin_convertvector(v, bf16x2);
  return __builtin_bit_cast(unsigned, r);
}
__device__ __forceinline__ float xrow_max(float x) {
  auto s = __builtin_amdgcn_permlane16_swap(__float_as_uint(x), __float_as_uint(x), false, false);
  x = fmaxf(__uint_as_float(s[0]), __uint_as_float(s[1]));
  auto t = __builtin_amdgcn_permlane32_swap(__float_as_uint(x), __float_as_uint(x), false, false);
  return fmaxf(__uint_as_float(t[0]), __uint_as_float(t[1]));
}
__device__ __forceinline__ float xrow_sum(float x) {
  auto s = __builtin_amdgcn_permlane16_swap(__float_as_uint(x), __float_as_uint(x), false, false);
  x = __uint_as_float(s[0]) + __uint_as_float(s[1]);
  auto t = __builtin_amdgcn_permlane32_swap(__float_as_uint(x), __float_as_uint(x), false, false);
  return __uint_as_float(t[0]) + __uint_as_float(t[1]);
}
__device__ __forceinline__ float lo2f(unsigned x) { return __uint_as_float(x << 16); }
__device__ __forceinline__ float hi2f(unsigned x) { return __uint_as_float(x & 0xffff0000u); }

struct State { float m, l; f32x4 acc[8]; };

__device__ __forceinline__ float red4(f32x4 d) { return ((d[0] + dpp0<0x104>(d[1])) + dpp0<0x108>(d[2])) + dpp0<0x10C>(d[3]); }

__device__ __forceinline__ void qk(const u32x4 (&K)[8], u32x4 qf, f32x4 (&d)[8]) {
#pragma unroll
  for (int i = 0; i < 8; ++i) d[i] = mfma(K[i], qf, f32x4{0.f, 0.f, 0.f, 0.f});
}
__device__ __forceinline__ u32x4 softmax_step(const f32x4 (&d)[8], State& st) {
  constexpr float C2 = SCALE * LOG2E;
  float s[8];
#pragma unroll
  for (int i = 0; i < 8; ++i) s[i] = red4(d[i]);
  float mx = fmaxf(fmaxf(fmaxf(s[0], s[1]), fmaxf(s[2], s[3])), fmaxf(fmaxf(s[4], s[5]), fmaxf(s[6], s[7])));
  mx = xrow_max(mx) * C2;
  const float mn = fmaxf(st.m, mx);
  const float alpha = __builtin_amdgcn_exp2f(st.m - mn);
  st.m = mn;
  float p[8];
#pragma unroll
  for (int i = 0; i < 8; ++i) p[i] = __builtin_amdgcn_exp2f(fmaf(s[i], C2, -mn));
  st.l = fmaf(st.l, alpha, ((p[0] + p[1]) + (p[2] + p[3])) + ((p[4] + p[5]) + (p[6] + p[7])));
#pragma unroll
  for (int e = 0; e < 8; ++e) st.acc[e] *= alpha;
  u32x4 pf = {cvtpk(p[0], p[1]), cvtpk(p[2], p[3]), cvtpk(p[4], p[5]), cvtpk(p[6], p[7])};
  return pf;
}
__device__ __forceinline__ void pv(const u32x4 (&V)[8], u32x4 pf, State& st) {
#pragma unroll
  for (int e = 0; e < 8; ++e) {
    const unsigned sel = (e & 1) ? 0x07060302u : 0x05040100u;
    u32x4 vf;
#pragma unroll
    for (int t = 0; t < 4; ++t) vf[t] = __builtin_amdgcn_perm(V[2 * t + 1][e >> 1], V[2 * t][e >> 1], sel);
    st.acc[e] = mfma(vf, pf, st.acc[e]);
  }
}
__device__ __forceinline__ unsigned tile_off(int t) { return (unsigned)(t >> 2) * 32768u + (unsigned)(t & 3) * 1024u; }
#define THR 63
#define STR_(x) #x
#define STR(x) STR_(x)
template <int AUX> __device__ __forceinline__ void load_tile_(u32x4 (&X)[8], __amdgpu_buffer_rsrc_t r, unsigned voff) {
  if (THR < 63) { asm volatile("s_waitcnt vmcnt(" STR(THR) ")" ::: "memory"); __builtin_amdgcn_sched_barrier(0); }
#pragma unroll
  for (int i = 0; i < 8; ++i) X[i] = __builtin_amdgcn_raw_buffer_load_b128(r, voff, i * 4096, AUX);
  asm volatile("" ::: "memory");
  __builtin_amdgcn_sched_barrier(0);
}
__global__ void __launch_bounds__(64, 2)
attn_kernel(const unsigned char* __restrict__ q, const unsigned char* kc, const unsigned char* vc,
            const unsigned char* __restrict__ kt, const unsigned char* __restrict__ vt, const unsigned short* __restrict__ mask,
            unsigned char* ws, unsigned char* out) {
  STAMP(0);
  const int grp = blockIdx.x / NSPLIT, gw = blockIdx.x % NSPLIT;
  const int lane = threadIdx.x, g = lane >> 4, c = lane & 15;
  const __amdgpu_buffer_rsrc_t rk = __builtin_amdgcn_make_buffer_rsrc((void*)(kc + (size_t)grp * GRP_BYTES), 0, GRP_BYTES, 0x00020000);
  const __amdgpu_buffer_rsrc_t rv = __builtin_amdgcn_make_buffer_rsrc((void*)(vc + (size_t)grp * GRP_BYTES), 0, GRP_BYTES, 0x00020000);
  const unsigned kpat = (c >> 2) * 256 + g * 64 + (c & 3) * 16, vpat = lane * 16;
  u64* const ctrl = reinterpret_cast<u64*>(ws + WS_CTRL + (size_t)grp * 256);

  __shared__ __attribute__((aligned(16))) float sc_lds[NPH * 128 + 512];
  u32x4 X0[8], X1[8], X2[8], X3[8];
  const u32x4 qf = *reinterpret_cast<const u32x4*>(q + ((size_t)grp * G + (c & 3)) * (D * 2) + ((c >> 2) + 4 * g) * 16);
  __builtin_amdgcn_sched_barrier(0);
  const unsigned off0 = tile_off(gw);
  load_tile_<AUXK>(X0, rk, off0 + kpat);
  load_tile_<AUXK>(X1, rk, off0 + 65536 + kpat);
  const u32x4 ktf = *reinterpret_cast<const u32x4*>(kt + (size_t)grp * (D * 2) + ((c & 3) + 4 * g) * 16);
  u32x4 vtf[4];
#pragma unroll
  for (int r = 0; r < 4; ++r) vtf[r] = *reinterpret_cast<const u32x4*>(vt + (size_t)grp * (D * 2) + g * 64 + r * 16);
  const unsigned mk = (reinterpret_cast<const unsigned*>(mask)[grp / (2 * HKV)] >> (((grp / HKV) & 1) * 16)) & 0xffffu;
  const u64 epoch_v = __hip_atomic_load(ctrl, __ATOMIC_RELAXED, __HIP_MEMORY_SCOPE_AGENT);
  const u64 w0 = __hip_atomic_load(ctrl + 16, __ATOMIC_RELAXED, __HIP_MEMORY_SCOPE_AGENT);
  __builtin_amdgcn_sched_barrier(0);
  load_tile_<AUXK>(X2, rk, off0 + 2 * 65536 + kpat);
  load_tile_<AUXK>(X3, rk, off0 + 3 * 65536 + kpat);
  STAMP(1);

  f32x4 d[8];
  float mx = NEG_MAX;
  {
    float* wr = sc_lds + (c < G ? (g * 4 + c) * 8 : NPH * 128 + lane * 8);
    const int wstep = c < G ? 128 : 0;
#define KSTEP(X, i_) do { \
      qk(X, qf, d); \
      __builtin_amdgcn_sched_barrier(0); \
      load_tile_<AUXK>(X, rs, voff + (i_) * 65536); \
      float s_[8]; \
      _Pragma("unroll") for (int i = 0; i < 8; ++i) s_[i] = red4(d[i]); \
      mx = fmaxf(fmaxf(fmaxf(mx, s_[0]), fmaxf(s_[1], s_[2])), fmaxf(fmaxf(fmaxf(s_[3], s_[4]), s_[5]), fmaxf(s_[6], s_[7]))); \
      *reinterpret_cast<f32x4*>(wr) = f32x4{s_[0], s_[1], s_[2], s_[3]}; \
      *reinterpret_cast<f32x4*>(wr + 4) = f32x4{s_[4], s_[5], s_[6], s_[7]}; \
      wr += wstep; \
      __builtin_amdgcn_sched_barrier(0); } while (0)
#pragma unroll 1
    for (int j = 4; j <= NPH; j += 4) {
      const bool more = j < NPH;
      const __amdgpu_buffer_rsrc_t rs = __builtin_amdgcn_make_buffer_rsrc((void*)((more ? kc : vc) + (size_t)grp * GRP_BYTES), 0, GRP_BYTES, 0x00020000);
      const unsigned voff = more ? off0 + j * 65536 + kpat : off0 + vpat;
      KSTEP(X0, 0); KSTEP(X1, 1); KSTEP(X2, 2); KSTEP(X3, 3);
    }
  }

  const u64 epoch = ((u64)(unsigned)__builtin_amdgcn_readfirstlane((unsigned)(epoch_v >> 32)) << 32) | (unsigned)__builtin_amdgcn_readfirstlane((unsigned)epoch_v);
  const u64 tag = epoch + 1;
  u64 claim = w0;
  if (lane == 0 && (w0 >> 16) != (tag & 0xffffffffffffull))
    __hip_atomic_compare_exchange_strong(ctrl + 16, &claim, tag << 16, __ATOMIC_RELAXED, __ATOMIC_RELAXED, __HIP_MEMORY_SCOPE_AGENT);
  const __amdgpu_buffer_rsrc_t rt = __builtin_amdgcn_make_buffer_rsrc((void*)(ctrl + 16), 0, 8, 0x00020000);
  const unsigned toff = lane == 0 ? 0u : 0x40000000u;

  constexpr float C2 = SCALE * LOG2E;
  State st;
  {
    const float stree = red4(mfma(ktf, qf, f32x4{0.f, 0.f, 0.f, 0.f}));
    const float tt = (stree * SCALE + (1.0f - lo2f(mk)) * -1.0e9f) * LOG2E;
    const bool seed = (gw == 0);
    mx = xrow_max(mx) * C2;
    st.m = fmaxf(seed ? fmaxf(tt, mx) : mx, NEG_MAX);
    const float pt = seed ? __builtin_amdgcn_exp2f(tt - st.m) : 0.f;
    st.l = g == 0 ? pt : 0.f;
#pragma unroll
    for (int e = 0; e < 8; ++e)
#pragma unroll
      for (int r = 0; r < 4; ++r) { const float x = (e & 1) ? hi2f(vtf[r][e >> 1]) : lo2f(vtf[r][e >> 1]); st.acc[e][r] = seed ? pt * x : 0.f; }
  }
  __builtin_amdgcn_sched_barrier(0);

  int tk = 0;
  {
    const float* rd = sc_lds + (g * 4 + (c & 3)) * 8;
#define VSTEP(X, rs_, voff_, TK) do { \
      const f32x4 s0 = *reinterpret_cast<const f32x4*>(rd), s1 = *reinterpret_cast<const f32x4*>(rd + 4); \
      rd += 128; \
      float p_[8]; \
      _Pragma("unroll") for (int i = 0; i < 4; ++i) { p_[i] = __builtin_amdgcn_exp2f(fmaf(s0[i], C2, -st.m)); p_[4 + i] = __builtin_amdgcn_exp2f(fmaf(s1[i], C2, -st.m)); } \
      st.l += ((p_[0] + p_[1]) + (p_[2] + p_[3])) + ((p_[4] + p_[5]) + (p_[6] + p_[7])); \
      asm volatile("" : "+v"(st.l)); \
      const u32x4 pf = {cvtpk(p_[0], p_[1]), cvtpk(p_[2], p_[3]), cvtpk(p_[4], p_[5]), cvtpk(p_[6], p_[7])}; \
      pv(X, pf, st); \
      __builtin_amdgcn_sched_barrier(0); \
      if (TK) { tk = __builtin_amdgcn_raw_ptr_buffer_atomic_add_i32(1, rt, toff | ((!more && NIL == 0) ? 0u : 0x40000000u), 0, 0); __builtin_amdgcn_sched_barrier(0); } \
      load_tile_<AUXV>(X, rs_, voff_); } while (0)
#pragma unroll 1
    for (int j = 4; j <= NPH; j += 4) {
      const bool more = j < NPH;
      const __amdgpu_buffer_rsrc_t rs = __builtin_amdgcn_make_buffer_rsrc((void*)((more ? vc : kc) + (size_t)grp * GRP_BYTES), 0, GRP_BYTES, 0x00020000);
      const unsigned vo = off0 + j * 65536 + vpat, ko = off0 + j * 65536 + (more ? vpat : kpat);
      const unsigned st1 = more ? 65536 : 0, st2 = more ? 2 * 65536 : 65536, st3 = more ? 3 * 65536 : 65536;
      VSTEP(X0, rs, ko, 0); VSTEP(X1, rv, vo + st1, 0); VSTEP(X2, rs, ko + st2, 1); VSTEP(X3, rv, vo + st3, 0);
      asm volatile("" :: "v"(claim));
    }
  }
  STAMP(3);

  int ta, tb = 0;
  int n = NPH + 2;
  int last = 0;
#define NEXT(tx) do { \
    if (n < NSTAT) last = gw + n * NSPLIT; \
    else if (last < NTILE) last = NSTAT * NSPLIT + (__builtin_amdgcn_readfirstlane(tk) & 0xffff); \
    tx = last; ++n; \
    tk = __builtin_amdgcn_raw_ptr_buffer_atomic_add_i32(1, rt, toff | ((n >= NSTAT && last < NTILE) ? 0u : 0x40000000u), 0, 0); \
    __builtin_amdgcn_sched_barrier(0); } while (0)
#pragma unroll 1
  do {
    qk(X0, qf, d);
    __builtin_amdgcn_sched_barrier(0);
    NEXT(ta);
    unsigned off = tile_off(ta);
    load_tile_<AUXK>(X0, rk, off + kpat);
    { u32x4 pf = softmax_step(d, st); pv(X1, pf, st); }
    __builtin_amdgcn_sched_barrier(0);
    load_tile_<AUXV>(X1, rv, off + vpat);
    const bool bv = tb < NTILE;
    if (bv) qk(X2, qf, d);
    __builtin_amdgcn_sched_barrier(0);
    NEXT(tb);
    off = tile_off(tb);
    load_tile_<AUXK>(X2, rk, off + kpat);
    if (bv) { u32x4 pf = softmax_step(d, st); pv(X3, pf, st); }
    __builtin_amdgcn_sched_barrier(0);
    load_tile_<AUXV>(X3, rv, off + vpat);
  } while (ta < NTILE);
  STAMP(2);

  float o[8], pm, pl;
#pragma unroll
  for (int e = 0; e < 8; ++e)
    o[e] = dpp_into<0x11C, 8>(dpp_into<0x118, 4>(dpp_into<0x114, 2>(st.acc[e][0], st.acc[e][1]), st.acc[e][2]), st.acc[e][3]);
  {
    const float ls = xrow_sum(st.l);
    pm = dpp_into<0x11C, 8>(dpp_into<0x118, 4>(dpp_into<0x114, 2>(st.m, st.m), st.m), st.m);
    pl = dpp_into<0x11C, 8>(dpp_into<0x118, 4>(dpp_into<0x114, 2>(ls, ls), ls), ls);
  }
  const unsigned tag0 = (unsigned)tag ^ 0x9e3779b9u, tag1 = (unsigned)(tag >> 32) ^ 0x5bd1e995u;
  const __amdgpu_buffer_rsrc_t rp = __builtin_amdgcn_make_buffer_rsrc((void*)(ws + WS_PART + (size_t)grp * NSPLIT * REC_BYTES), 0, NSPLIT * REC_BYTES, 0x00020000);
  if (ta != NTILE + NSPLIT - 1) {
    const u32x4 w0v = {__float_as_uint(o[0]), __float_as_uint(o[1]), __float_as_uint(o[2]), __float_as_uint(o[3])};
    const u32x4 w1v = {__float_as_uint(o[4]), __float_as_uint(o[5]), __float_as_uint(o[6]), __float_as_uint(o[7])};
    const u32x4 w2v = {__float_as_uint(pm), __float_as_uint(pl), tag0 ^ w0v[0] ^ w0v[2] ^ w1v[0] ^ w1v[2] ^ __float_as_uint(pm),
                       tag1 ^ w0v[1] ^ w0v[3] ^ w1v[1] ^ w1v[3] ^ __float_as_uint(pl)};
    __builtin_amdgcn_raw_buffer_store_b128(w0v, rp, vpat, gw * REC_BYTES, 16);
    __builtin_amdgcn_raw_buffer_store_b128(w1v, rp, vpat + 1024, gw * REC_BYTES, 16);
    __builtin_amdgcn_raw_buffer_store_b128(w2v, rp, vpat + 2048, gw * REC_BYTES, 16);
    STAMP_FLUSH();
    return;
  }
  u32x4 R[NSPLIT - 1][3];
#pragma unroll 1
  for (int spin = 0; spin < (1 << 18); ++spin) {
#pragma unroll
    for (int j = 0; j < NSPLIT - 1; ++j) {
      const int s = (gw + 1 + j) & (NSPLIT - 1);
#pragma unroll
      for (int k = 0; k < 3; ++k) R[j][k] = __builtin_amdgcn_raw_buffer_load_b128(rp, vpat + k * 1024, s * REC_BYTES, 16);
    }
    asm volatile("" ::: "memory");
    unsigned bad = 0;
#pragma unroll
    for (int j = 0; j < NSPLIT - 1; ++j) {
      bad |= tag0 ^ R[j][0][0] ^ R[j][0][2] ^ R[j][1][0] ^ R[j][1][2] ^ R[j][2][0] ^ R[j][2][2];
      bad |= tag1 ^ R[j][0][1] ^ R[j][0][3] ^ R[j][1][1] ^ R[j][1][3] ^ R[j][2][1] ^ R[j][2][3];
    }
    if (__builtin_amdgcn_ballot_w64(bad != 0) == 0) break;
  }
  STAMP(4);
  float M = pm;
#pragma unroll
  for (int j = 0; j < NSPLIT - 1; ++j) M = fmaxf(M, __uint_as_float(R[j][2][0]));
  {
    const float w = __builtin_amdgcn_exp2f(pm - M);
    pl *= w;
#pragma unroll
    for (int e = 0; e < 8; ++e) o[e] *= w;
  }
#pragma unroll
  for (int j = 0; j < NSPLIT - 1; ++j) {
    const float w = __builtin_amdgcn_exp2f(__uint_as_float(R[j][2][0]) - M);
    pl = fmaf(w, __uint_as_float(R[j][2][1]), pl);
#pragma unroll
    for (int e = 0; e < 8; ++e) o[e] = fmaf(w, __uint_as_float(R[j][e >> 2][e & 3]), o[e]);
  }
  const float inv = 1.0f / pl;
  const u32x4 res = {cvtpk(o[0] * inv, o[1] * inv), cvtpk(o[2] * inv, o[3] * inv), cvtpk(o[4] * inv, o[5] * inv), cvtpk(o[6] * inv, o[7] * inv)};
  *reinterpret_cast<u32x4*>(out + ((size_t)grp * G + (c & 3)) * (D * 2) + (4 * g + (c >> 2)) * 16) = res;
  if (lane == 0) __hip_atomic_store(ctrl, tag, __ATOMIC_RELAXED, __HIP_MEMORY_SCOPE_AGENT);
  STAMP(5);
  STAMP_FLUSH();
}

extern "C" void kernel_launch(void* const* d_in, const int* in_sizes, int n_in, void* d_out, int out_size, void* d_ws,
                              size_t ws_size, hipStream_t stream) {
  (void)in_sizes; (void)n_in; (void)out_size; (void)ws_size;
  attn_kernel<<<NGRP * NSPLIT, 64, LDSPAD, stream>>>((const unsigned char*)d_in[0], (const unsigned char*)d_in[1], (const unsigned char*)d_in[2],
                                                     (const unsigned char*)d_in[3], (const unsigned char*)d_in[4],
                                                     (const unsigned short*)d_in[5], (unsigned char*)d_ws, (unsigned char*)d_out);
}
```

```cpp
#include <hip/hip_runtime.h>

__global__ void __launch_bounds__(64, 1)
attn_kernel(const void* q, const void* kc, const void* vc, const void* kt, const void* vt, const void* mask, void* ws, void* out) {
  __shared__ __attribute__((aligned(16))) float scores[32 * 128];
  asm volatile(
      "s_load_dwordx16 s[4:19], %0, 0x0\n"
      "s_memrealtime s[54:55]\n"
      "s_lshr_b32 s43, %1, 3\n"
      "s_and_b32 s44, %1, 7\n"
      "v_lshlrev_b32 v2, 4, %2\n"
      "s_getpc_b64 s[56:57]\n"
      "pcref_%=:\n"
      "s_and_b32 s47, s43, 15\n"
      "s_lshl_b32 s47, s47, 10\n"
      "s_min_u32 s47, s47, done_%=-pcref_%=-1024\n"
      "s_add_u32 s56, s56, s47\n"
      "s_addc_u32 s57, s57, 0\n"
      "global_load_dwordx4 v[96:99], v2, s[56:57]\n"
      "v_lshrrev_b32 v20, 4, %2\n"
      "v_bfe_u32 v21, %2, 2, 2\n"
      "v_and_b32 v22, 3, %2\n"
      "v_lshlrev_b32 v23, 6, v20\n"
      "v_lshl_or_b32 v36, v22, 4, v23\n"
      "v_lshl_or_b32 v1, v21, 8, v36\n"
      "v_lshl_or_b32 v4, v21, 4, v23\n"
      "v_lshl_or_b32 v4, v22, 8, v4\n"
      "v_lshl_or_b32 v6, v20, 2, v22\n"
      "v_lshl_add_u32 v6, v6, 5, %3\n"
      "v_mov_b32 v5, v6\n"
      "v_mov_b32 v9, 0\n"
      "v_mov_b32 v7, 0xff7fffff\n"
      "s_movk_i32 s36, 0x1000\n"
      "s_movk_i32 s37, 0x2000\n"
      "s_movk_i32 s38, 0x3000\n"
      "s_movk_i32 s39, 0x4000\n"
      "s_movk_i32 s40, 0x5000\n"
      "s_movk_i32 s41, 0x6000\n"
      "s_movk_i32 s42, 0x7000\n"
      "s_lshl_b32 s47, s44, 13\n"
      "s_and_b32 s47, s47, 0x8000\n"
      "s_lshl_b32 s48, s44, 10\n"
      "s_and_b32 s48, s48, 0xc00\n"
      "s_or_b32 s46, s47, s48\n"
      "v_add_u32 v3, s46, v1\n"
      "s_mov_b32 s22, 0x200000\n"
      "s_mov_b32 s23, 0x20000\n"
      "s_lshl_b32 s47, s43, 21\n"
      "s_lshl_b32 s48, s43, 10\n"
      "s_lshl_b32 s56, s43, 8\n"
      "s_and_b32 s67, s43, 15\n"
      "s_mul_i32 s67, s67, 212\n"
      "s_lshr_b32 s67, s67, 6\n"
      "s_add_u32 s67, s67, 49\n"
      "s_lshr_b32 s63, s43, 4\n"
      "s_lshl_b32 s63, s63, 2\n"
      "s_waitcnt lgkmcnt(0)\n"
      "s_memrealtime s[52:53]\n"
      "s_add_u32 s20, s6, s47\n"
      "s_addc_u32 s21, s7, 0\n"
      "s_and_b32 s21, s21, 0xffff\n"
      "s_add_u32 s4, s4, s48\n"
      "s_addc_u32 s5, s5, 0\n"
      "s_load_dword s69, s[8:9], s47\n"
      "s_load_dword s69, s[4:5], 0x0\n"
      "s_load_dword s69, s[16:17], 0xf000\n"
      "s_cmp_lg_u32 s44, 0\n"
      "s_cbranch_scc1 nomask_%=\n"
      "s_load_dword s63, s[14:15], s63\n"
      "nomask_%=:\n"
      "buffer_load_dwordx4 v[128:131], v3, s[20:23], 0 offen nt\n"
      "buffer_load_dwordx4 v[132:135], v3, s[20:23], s36 offen nt\n"
      "buffer_load_dwordx4 v[136:139], v3, s[20:23], s37 offen nt\n"
      "buffer_load_dwordx4 v[140:143], v3, s[20:23], s38 offen nt\n"
      "buffer_load_dwordx4 v[144:147], v3, s[20:23], s39 offen nt\n"
      "buffer_load_dwordx4 v[148:151], v3, s[20:23], s40 offen nt\n"
      "buffer_load_dwordx4 v[152:155], v3, s[20:23], s41 offen nt\n"
      "buffer_load_dwordx4 v[156:159], v3, s[20:23], s42 offen nt\n"
      "v_add_u32 v3, 0x10000, v3\n"
      "s_waitcnt lgkmcnt(0)\n"
      "s_memrealtime s[58:59]\n"
      "s_add_u32 s67, s67, s52\n"
      "s_sub_u32 s68, s52, s54\n"
      "s_cmp_gt_u32 s68, 55\n"
      "s_cselect_b32 s26, 20, 10\n"
      "s_cselect_b32 s68, 20, 0\n"
      "s_add_u32 s67, s67, s68\n"
      "global_load_dwordx4 v[16:19], v4, s[4:5]\n"
      "pc0_%=:\n"
      "s_waitcnt lgkmcnt(0)\n"
      "s_sub_u32 s58, s58, s67\n"
      "s_cmp_ge_i32 s58, 0\n"
      "s_cbranch_scc1 pd0_%=\n"
      "s_memrealtime s[58:59]\n"
      "s_branch pc0_%=\n"
      "pd0_%=:\n"
      "s_add_u32 s67, s67, 53\n"
      "buffer_load_dwordx4 v[160:163], v3, s[20:23], 0 offen nt\n"
      "buffer_load_dwordx4 v[164:167], v3, s[20:23], s36 offen nt\n"
      "buffer_load_dwordx4 v[168:171], v3, s[20:23], s37 offen nt\n"
      "buffer_load_dwordx4 v[172:175], v3, s[20:23], s38 offen nt\n"
      "buffer_load_dwordx4 v[176:179], v3, s[20:23], s39 offen nt\n"
      "buffer_load_dwordx4 v[180:183], v3, s[20:23], s40 offen nt\n"
      "buffer_load_dwordx4 v[184:187], v3, s[20:23], s41 offen nt\n"
      "buffer_load_dwordx4 v[188:191], v3, s[20:23], s42 offen nt\n"
      "v_add_u32 v3, 0x10000, v3\n"
      "s_memrealtime s[58:59]\n"
      "pc1_%=:\n"
      "s_waitcnt lgkmcnt(0)\n"
      "s_sub_u32 s58, s58, s67\n"
      "s_cmp_ge_i32 s58, 0\n"
      "s_cbranch_scc1 pd1_%=\n"
      "s_memrealtime s[58:59]\n"
      "s_branch pc1_%=\n"
      "pd1_%=:\n"
      "s_add_u32 s67, s67, 53\n"
      "buffer_load_dwordx4 v[192:195], v3, s[20:23], 0 offen nt\n"
      "buffer_load_dwordx4 v[196:199], v3, s[20:23], s36 offen nt\n"
      "buffer_load_dwordx4 v[200:203], v3, s[20:23], s37 offen nt\n"
      "buffer_load_dwordx4 v[204:207], v3, s[20:23], s38 offen nt\n"
      "buffer_load_dwordx4 v[208:211], v3, s[20:23], s39 offen nt\n"
      "buffer_load_dwordx4 v[212:215], v3, s[20:23], s40 offen nt\n"
      "buffer_load_dwordx4 v[216:219], v3, s[20:23], s41 offen nt\n"
      "buffer_load_dwordx4 v[220:223], v3, s[20:23], s42 offen nt\n"
      "v_add_u32 v3, 0x10000, v3\n"
      "s_memrealtime s[58:59]\n"
      "pc2_%=:\n"
      "s_waitcnt lgkmcnt(0)\n"
      "s_sub_u32 s58, s58, s67\n"
      "s_cmp_ge_i32 s58, 0\n"
      "s_cbranch_scc1 pd2_%=\n"
      "s_memrealtime s[58:59]\n"
      "s_branch pc2_%=\n"
      "pd2_%=:\n"
      "s_add_u32 s67, s67, 53\n"
      "buffer_load_dwordx4 v[224:227], v3, s[20:23], 0 offen nt\n"
      "buffer_load_dwordx4 v[228:231], v3, s[20:23], s36 offen nt\n"
      "buffer_load_dwordx4 v[232:235], v3, s[20:23], s37 offen nt\n"
      "buffer_load_dwordx4 v[236:239], v3, s[20:23], s38 offen nt\n"
      "buffer_load_dwordx4 v[240:243], v3, s[20:23], s39 offen nt\n"
      "buffer_load_dwordx4 v[244:247], v3, s[20:23], s40 offen nt\n"
      "buffer_load_dwordx4 v[248:251], v3, s[20:23], s41 offen nt\n"
      "buffer_load_dwordx4 v[252:255], v3, s[20:23], s42 offen nt\n"
      "v_add_u32 v3, 0x10000, v3\n"
      "s_add_u32 s50, s16, s56\n"
      "s_addc_u32 s51, s17, 0\n"
      "global_load_dwordx2 v[56:57], v9, s[50:51] sc1\n"
      "global_load_dwordx2 v[58:59], v9, s[50:51] offset:128 sc1\n"
      "s_add_u32 s24, s8, s47\n"
      "s_addc_u32 s25, s9, 0\n"
      "s_and_b32 s25, s25, 0xffff\n"
      "s_add_u32 s18, s18, s48\n"
      "s_addc_u32 s19, s19, 0\n"
      "s_cmp_lg_u32 s44, 0\n"
      "s_cbranch_scc1 noseedld_%=\n"
      "s_add_u32 s10, s10, s56\n"
      "s_addc_u32 s11, s11, 0\n"
      "s_add_u32 s12, s12, s56\n"
      "s_addc_u32 s13, s13, 0\n"
      "global_load_dwordx4 v[52:55], v36, s[10:11]\n"
      "global_load_dwordx4 v[80:83], v23, s[12:13] offset:0\n"
      "global_load_dwordx4 v[84:87], v23, s[12:13] offset:16\n"
      "global_load_dwordx4 v[88:91], v23, s[12:13] offset:32\n"
      "global_load_dwordx4 v[92:95], v23, s[12:13] offset:48\n"
      "noseedld_%=:\n"
      "s_lshl_b32 s47, s43, 15\n"
      "s_add_u32 s47, s47, 0x10000\n"
      "s_add_u32 s28, s16, s47\n"
      "s_addc_u32 s29, s17, 0\n"
      "s_and_b32 s29, s29, 0xffff\n"
      "s_mov_b32 s30, 0x8000\n"
      "s_mov_b32 s31, 0x20000\n"
      "s_mov_b32 s60, 0x3e0293ee\n"
      "s_mov_b32 s61, 0x5040100\n"
      "s_mov_b32 s62, 0x7060302\n"
      "s_mov_b32 s54, 0x000f000f\n"
      "s_mov_b32 s55, 0x000f000f\n"
      "s_mov_b32 s45, 0\n"
      "s_mov_b32 s49, 0\n"
      "s_mov_b32 s27, 0\n"
      "s_mov_b32 s66, 0x40000\n"
      "kloop_%=:\n"
      "s_waitcnt vmcnt(24)\n"
      "s_memrealtime s[58:59]\n"
      "v_mfma_f32_16x16x32_bf16 v[96:99], v[128:131], v[16:19], 0\n"
      "v_mfma_f32_16x16x32_bf16 v[100:103], v[132:135], v[16:19], 0\n"
      "v_mfma_f32_16x16x32_bf16 v[104:107], v[136:139], v[16:19], 0\n"
      "v_mfma_f32_16x16x32_bf16 v[108:111], v[140:143], v[16:19], 0\n"
      "v_mfma_f32_16x16x32_bf16 v[112:115], v[144:147], v[16:19], 0\n"
      "v_mfma_f32_16x16x32_bf16 v[116:119], v[148:151], v[16:19], 0\n"
      "v_mfma_f32_16x16x32_bf16 v[120:123], v[152:155], v[16:19], 0\n"
      "v_mfma_f32_16x16x32_bf16 v[124:127], v[156:159], v[16:19], 0\n"
      "s_cmp_eq_u32 s45, 4\n"
      "s_cbranch_scc1 kmean_%=\n"
      "kgo_%=:\n"
      "s_add_u32 s67, s67, s27\n"
      "s_cmp_lg_u32 s45, 7\n"
      "s_cbranch_scc1 kmore_%=\n"
      "s_mov_b64 s[20:21], s[24:25]\n"
      "v_add_u32 v3, s46, v2\n"
      "s_add_u32 s67, s67, s26\n"
      "kmore_%=:\n"
      "pc3_%=:\n"
      "s_waitcnt lgkmcnt(0)\n"
      "s_sub_u32 s58, s58, s67\n"
      "s_cmp_ge_i32 s58, 0\n"
      "s_cbranch_scc1 pd3_%=\n"
      "s_memrealtime s[58:59]\n"
      "s_branch pc3_%=\n"
      "pd3_%=:\n"
      "s_add_u32 s67, s67, 53\n"
      "buffer_load_dwordx4 v[128:131], v3, s[20:23], 0 offen nt\n"
      "buffer_load_dwordx4 v[132:135], v3, s[20:23], s36 offen nt\n"
      "buffer_load_dwordx4 v[136:139], v3, s[20:23], s37 offen nt\n"
      "buffer_load_dwordx4 v[140:143], v3, s[20:23], s38 offen nt\n"
      "buffer_load_dwordx4 v[144:147], v3, s[20:23], s39 offen nt\n"
      "buffer_load_dwordx4 v[148:151], v3, s[20:23], s40 offen nt\n"
      "buffer_load_dwordx4 v[152:155], v3, s[20:23], s41 offen nt\n"
      "buffer_load_dwordx4 v[156:159], v3, s[20:23], s42 offen nt\n"
      "v_add_u32 v3, 0x10000, v3\n"
      "s_cmp_eq_u32 s45, 1\n"
      "s_cbranch_scc1 kclaim_%=\n"
      "s_cmp_eq_u32 s45, 2\n"
      "s_cbranch_scc1 kdist_%=\n"
      "s_cmp_lg_u32 s45, 3\n"
      "s_cbranch_scc1 slot_%=\n"
      "s_mov_b64 exec, 1\n"
      "global_load_dword v12, v9, s[50:51] offset:128 sc1\n"
      "s_mov_b64 exec, -1\n"
      "slot_%=:\n"
      "v_add_f32_dpp v96, v97, v96 row_shl:4 row_mask:0xf bank_mask:0xf bound_ctrl:1\n"
      "v_add_f32_dpp v100, v101, v100 row_shl:4 row_mask:0xf bank_mask:0xf bound_ctrl:1\n"
      "v_add_f32_dpp v104, v105, v104 row_shl:4 row_mask:0xf bank_mask:0xf bound_ctrl:1\n"
      "v_add_f32_dpp v108, v109, v108 row_shl:4 row_mask:0xf bank_mask:0xf bound_ctrl:1\n"
      "v_add_f32_dpp v112, v113, v112 row_shl:4 row_mask:0xf bank_mask:0xf bound_ctrl:1\n"
      "v_add_f32_dpp v116, v117, v116 row_shl:4 row_mask:0xf bank_mask:0xf bound_ctrl:1\n"
      "v_add_f32_dpp v120, v121, v120 row_shl:4 row_mask:0xf bank_mask:0xf bound_ctrl:1\n"
      "v_add_f32_dpp v124, v125, v124 row_shl:4 row_mask:0xf bank_mask:0xf bound_ctrl:1\n"
      "v_add_f32_dpp v96, v98, v96 row_shl:8 row_mask:0xf bank_mask:0xf bound_ctrl:1\n"
      "v_add_f32_dpp v100, v102, v100 row_shl:8 row_mask:0xf bank_mask:0xf bound_ctrl:1\n"
      "v_add_f32_dpp v104, v106, v104 row_shl:8 row_mask:0xf bank_mask:0xf bound_ctrl:1\n"
      "v_add_f32_dpp v108, v110, v108 row_shl:8 row_mask:0xf bank_mask:0xf bound_ctrl:1\n"
      "v_add_f32_dpp v112, v114, v112 row_shl:8 row_mask:0xf bank_mask:0xf bound_ctrl:1\n"
      "v_add_f32_dpp v116, v118, v116 row_shl:8 row_mask:0xf bank_mask:0xf bound_ctrl:1\n"
      "v_add_f32_dpp v120, v122, v120 row_shl:8 row_mask:0xf bank_mask:0xf bound_ctrl:1\n"
      "v_add_f32_dpp v124, v126, v124 row_shl:8 row_mask:0xf bank_mask:0xf bound_ctrl:1\n"
      "v_add_f32_dpp v24, v99, v96 row_shl:12 row_mask:0xf bank_mask:0xf bound_ctrl:1\n"
      "v_add_f32_dpp v25, v103, v100 row_shl:12 row_mask:0xf bank_mask:0xf bound_ctrl:1\n"
      "v_add_f32_dpp v26, v107, v104 row_shl:12 row_mask:0xf bank_mask:0xf bound_ctrl:1\n"
      "v_add_f32_dpp v27, v111, v108 row_shl:12 row_mask:0xf bank_mask:0xf bound_ctrl:1\n"
      "v_add_f32_dpp v28, v115, v112 row_shl:12 row_mask:0xf bank_mask:0xf bound_ctrl:1\n"
      "v_add_f32_dpp v29, v119, v116 row_shl:12 row_mask:0xf bank_mask:0xf bound_ctrl:1\n"
      "v_add_f32_dpp v30, v123, v120 row_shl:12 row_mask:0xf bank_mask:0xf bound_ctrl:1\n"
      "v_add_f32_dpp v31, v127, v124 row_shl:12 row_mask:0xf bank_mask:0xf bound_ctrl:1\n"
      "s_mov_b64 exec, s[54:55]\n"
      "ds_write_b128 v5, v[24:27] offset:0\n"
      "ds_write_b128 v5, v[28:31] offset:16\n"
      "s_mov_b64 exec, -1\n"
      "v_max3_f32 v7, v7, v24, v25\n"
      "v_max3_f32 v7, v7, v26, v27\n"
      "v_max3_f32 v7, v7, v28, v29\n"
      "v_max3_f32 v7, v7, v30, v31\n"
      "s_waitcnt vmcnt(24)\n"
      "s_memrealtime s[58:59]\n"
      "v_mfma_f32_16x16x32_bf16 v[96:99], v[160:163], v[16:19], 0\n"
      "v_mfma_f32_16x16x32_bf16 v[100:103], v[164:167], v[16:19], 0\n"
      "v_mfma_f32_16x16x32_bf16 v[104:107], v[168:171], v[16:19], 0\n"
      "v_mfma_f32_16x16x32_bf16 v[108:111], v[172:175], v[16:19], 0\n"
      "v_mfma_f32_16x16x32_bf16 v[112:115], v[176:179], v[16:19], 0\n"
      "v_mfma_f32_16x16x32_bf16 v[116:119], v[180:183], v[16:19], 0\n"
      "v_mfma_f32_16x16x32_bf16 v[120:123], v[184:187], v[16:19], 0\n"
      "v_mfma_f32_16x16x32_bf16 v[124:127], v[188:191], v[16:19], 0\n"
      "pc4_%=:\n"
      "s_waitcnt lgkmcnt(0)\n"
      "s_sub_u32 s58, s58, s67\n"
      "s_cmp_ge_i32 s58, 0\n"
      "s_cbranch_scc1 pd4_%=\n"
      "s_memrealtime s[58:59]\n"
      "s_branch pc4_%=\n"
      "pd4_%=:\n"
      "s_add_u32 s67, s67, 53\n"
      "buffer_load_dwordx4 v[160:163], v3, s[20:23], 0 offen nt\n"
      "buffer_load_dwordx4 v[164:167], v3, s[20:23], s36 offen nt\n"
      "buffer_load_dwordx4 v[168:171], v3, s[20:23], s37 offen nt\n"
      "buffer_load_dwordx4 v[172:175], v3, s[20:23], s38 offen nt\n"
      "buffer_load_dwordx4 v[176:179], v3, s[20:23], s39 offen nt\n"
      "buffer_load_dwordx4 v[180:183], v3, s[20:23], s40 offen nt\n"
      "buffer_load_dwordx4 v[184:187], v3, s[20:23], s41 offen nt\n"
      "buffer_load_dwordx4 v[188:191], v3, s[20:23], s42 offen nt\n"
      "v_add_u32 v3, 0x10000, v3\n"
      "v_add_f32_dpp v96, v97, v96 row_shl:4 row_mask:0xf bank_mask:0xf bound_ctrl:1\n"
      "v_add_f32_dpp v100, v101, v100 row_shl:4 row_mask:0xf bank_mask:0xf bound_ctrl:1\n"
      "v_add_f32_dpp v104, v105, v104 row_shl:4 row_mask:0xf bank_mask:0xf bound_ctrl:1\n"
      "v_add_f32_dpp v108, v109, v108 row_shl:4 row_mask:0xf bank_mask:0xf bound_ctrl:1\n"
      "v_add_f32_dpp v112, v113, v112 row_shl:4 row_mask:0xf bank_mask:0xf bound_ctrl:1\n"
      "v_add_f32_dpp v116, v117, v116 row_shl:4 row_mask:0xf bank_mask:0xf bound_ctrl:1\n"
      "v_add_f32_dpp v120, v121, v120 row_shl:4 row_mask:0xf bank_mask:0xf bound_ctrl:1\n"
      "v_add_f32_dpp v124, v125, v124 row_shl:4 row_mask:0xf bank_mask:0xf bound_ctrl:1\n"
      "v_add_f32_dpp v96, v98, v96 row_shl:8 row_mask:0xf bank_mask:0xf bound_ctrl:1\n"
      "v_add_f32_dpp v100, v102, v100 row_shl:8 row_mask:0xf bank_mask:0xf bound_ctrl:1\n"
      "v_add_f32_dpp v104, v106, v104 row_shl:8 row_mask:0xf bank_mask:0xf bound_ctrl:1\n"
      "v_add_f32_dpp v108, v110, v108 row_shl:8 row_mask:0xf bank_mask:0xf bound_ctrl:1\n"
      "v_add_f32_dpp v112, v114, v112 row_shl:8 row_mask:0xf bank_mask:0xf bound_ctrl:1\n"
      "v_add_f32_dpp v116, v118, v116 row_shl:8 row_mask:0xf bank_mask:0xf bound_ctrl:1\n"
      "v_add_f32_dpp v120, v122, v120 row_shl:8 row_mask:0xf bank_mask:0xf bound_ctrl:1\n"
      "v_add_f32_dpp v124, v126, v124 row_shl:8 row_mask:0xf bank_mask:0xf bound_ctrl:1\n"
      "v_add_f32_dpp v24, v99, v96 row_shl:12 row_mask:0xf bank_mask:0xf bound_ctrl:1\n"
      "v_add_f32_dpp v25, v103, v100 row_shl:12 row_mask:0xf bank_mask:0xf bound_ctrl:1\n"
      "v_add_f32_dpp v26, v107, v104 row_shl:12 row_mask:0xf bank_mask:0xf bound_ctrl:1\n"
      "v_add_f32_dpp v27, v111, v108 row_shl:12 row_mask:0xf bank_mask:0xf bound_ctrl:1\n"
      "v_add_f32_dpp v28, v115, v112 row_shl:12 row_mask:0xf bank_mask:0xf bound_ctrl:1\n"
      "v_add_f32_dpp v29, v119, v116 row_shl:12 row_mask:0xf bank_mask:0xf bound_ctrl:1\n"
      "v_add_f32_dpp v30, v123, v120 row_shl:12 row_mask:0xf bank_mask:0xf bound_ctrl:1\n"
      "v_add_f32_dpp v31, v127, v124 row_shl:12 row_mask:0xf bank_mask:0xf bound_ctrl:1\n"
      "s_mov_b64 exec, s[54:55]\n"
      "ds_write_b128 v5, v[24:27] offset:512\n"
      "ds_write_b128 v5, v[28:31] offset:528\n"
      "s_mov_b64 exec, -1\n"
      "v_max3_f32 v7, v7, v24, v25\n"
      "v_max3_f32 v7, v7, v26, v27\n"
      "v_max3_f32 v7, v7, v28, v29\n"
      "v_max3_f32 v7, v7, v30, v31\n"
      "s_waitcnt vmcnt(24)\n"
      "s_memrealtime s[58:59]\n"
      "v_mfma_f32_16x16x32_bf16 v[96:99], v[192:195], v[16:19], 0\n"
      "v_mfma_f32_16x16x32_bf16 v[100:103], v[196:199], v[16:19], 0\n"
      "v_mfma_f32_16x16x32_bf16 v[104:107], v[200:203], v[16:19], 0\n"
      "v_mfma_f32_16x16x32_bf16 v[108:111], v[204:207], v[16:19], 0\n"
      "v_mfma_f32_16x16x32_bf16 v[112:115], v[208:211], v[16:19], 0\n"
      "v_mfma_f32_16x16x32_bf16 v[116:119], v[212:215], v[16:19], 0\n"
      "v_mfma_f32_16x16x32_bf16 v[120:123], v[216:219], v[16:19], 0\n"
      "v_mfma_f32_16x16x32_bf16 v[124:127], v[220:223], v[16:19], 0\n"
      "pc5_%=:\n"
      "s_waitcnt lgkmcnt(0)\n"
      "s_sub_u32 s58, s58, s67\n"
      "s_cmp_ge_i32 s58, 0\n"
      "s_cbranch_scc1 pd5_%=\n"
      "s_memrealtime s[58:59]\n"
      "s_branch pc5_%=\n"
      "pd5_%=:\n"
      "s_add_u32 s67, s67, 53\n"
      "buffer_load_dwordx4 v[192:195], v3, s[20:23], 0 offen nt\n"
      "buffer_load_dwordx4 v[196:199], v3, s[20:23], s36 offen nt\n"
      "buffer_load_dwordx4 v[200:203], v3, s[20:23], s37 offen nt\n"
      "buffer_load_dwordx4 v[204:207], v3, s[20:23], s38 offen nt\n"
      "buffer_load_dwordx4 v[208:211], v3, s[20:23], s39 offen nt\n"
      "buffer_load_dwordx4 v[212:215], v3, s[20:23], s40 offen nt\n"
      "buffer_load_dwordx4 v[216:219], v3, s[20:23], s41 offen nt\n"
      "buffer_load_dwordx4 v[220:223], v3, s[20:23], s42 offen nt\n"
      "v_add_u32 v3, 0x10000, v3\n"
      "v_add_f32_dpp v96, v97, v96 row_shl:4 row_mask:0xf bank_mask:0xf bound_ctrl:1\n"
      "v_add_f32_dpp v100, v101, v100 row_shl:4 row_mask:0xf bank_mask:0xf bound_ctrl:1\n"
      "v_add_f32_dpp v104, v105, v104 row_shl:4 row_mask:0xf bank_mask:0xf bound_ctrl:1\n"
      "v_add_f32_dpp v108, v109, v108 row_shl:4 row_mask:0xf bank_mask:0xf bound_ctrl:1\n"
      "v_add_f32_dpp v112, v113, v112 row_shl:4 row_mask:0xf bank_mask:0xf bound_ctrl:1\n"
      "v_add_f32_dpp v116, v117, v116 row_shl:4 row_mask:0xf bank_mask:0xf bound_ctrl:1\n"
      "v_add_f32_dpp v120, v121, v120 row_shl:4 row_mask:0xf bank_mask:0xf bound_ctrl:1\n"
      "v_add_f32_dpp v124, v125, v124 row_shl:4 row_mask:0xf bank_mask:0xf bound_ctrl:1\n"
      "v_add_f32_dpp v96, v98, v96 row_shl:8 row_mask:0xf bank_mask:0xf bound_ctrl:1\n"
      "v_add_f32_dpp v100, v102, v100 row_shl:8 row_mask:0xf bank_mask:0xf bound_ctrl:1\n"
      "v_add_f32_dpp v104, v106, v104 row_shl:8 row_mask:0xf bank_mask:0xf bound_ctrl:1\n"
      "v_add_f32_dpp v108, v110, v108 row_shl:8 row_mask:0xf bank_mask:0xf bound_ctrl:1\n"
      "v_add_f32_dpp v112, v114, v112 row_shl:8 row_mask:0xf bank_mask:0xf bound_ctrl:1\n"
      "v_add_f32_dpp v116, v118, v116 row_shl:8 row_mask:0xf bank_mask:0xf bound_ctrl:1\n"
      "v_add_f32_dpp v120, v122, v120 row_shl:8 row_mask:0xf bank_mask:0xf bound_ctrl:1\n"
      "v_add_f32_dpp v124, v126, v124 row_shl:8 row_mask:0xf bank_mask:0xf bound_ctrl:1\n"
      "v_add_f32_dpp v24, v99, v96 row_shl:12 row_mask:0xf bank_mask:0xf bound_ctrl:1\n"
      "v_add_f32_dpp v25, v103, v100 row_shl:12 row_mask:0xf bank_mask:0xf bound_ctrl:1\n"
      "v_add_f32_dpp v26, v107, v104 row_shl:12 row_mask:0xf bank_mask:0xf bound_ctrl:1\n"
      "v_add_f32_dpp v27, v111, v108 row_shl:12 row_mask:0xf bank_mask:0xf bound_ctrl:1\n"
      "v_add_f32_dpp v28, v115, v112 row_shl:12 row_mask:0xf bank_mask:0xf bound_ctrl:1\n"
      "v_add_f32_dpp v29, v119, v116 row_shl:12 row_mask:0xf bank_mask:0xf bound_ctrl:1\n"
      "v_add_f32_dpp v30, v123, v120 row_shl:12 row_mask:0xf bank_mask:0xf bound_ctrl:1\n"
      "v_add_f32_dpp v31, v127, v124 row_shl:12 row_mask:0xf bank_mask:0xf bound_ctrl:1\n"
      "s_mov_b64 exec, s[54:55]\n"
      "ds_write_b128 v5, v[24:27] offset:1024\n"
      "ds_write_b128 v5, v[28:31] offset:1040\n"
      "s_mov_b64 exec, -1\n"
      "v_max3_f32 v7, v7, v24, v25\n"
      "v_max3_f32 v7, v7, v26, v27\n"
      "v_max3_f32 v7, v7, v28, v29\n"
      "v_max3_f32 v7, v7, v30, v31\n"
      "s_waitcnt vmcnt(24)\n"
      "s_memrealtime s[58:59]\n"
      "v_mfma_f32_16x16x32_bf16 v[96:99], v[224:227], v[16:19], 0\n"
      "v_mfma_f32_16x16x32_bf16 v[100:103], v[228:231], v[16:19], 0\n"
      "v_mfma_f32_16x16x32_bf16 v[104:107], v[232:235], v[16:19], 0\n"
      "v_mfma_f32_16x16x32_bf16 v[108:111], v[236:239], v[16:19], 0\n"
      "v_mfma_f32_16x16x32_bf16 v[112:115], v[240:243], v[16:19], 0\n"
      "v_mfma_f32_16x16x32_bf16 v[116:119], v[244:247], v[16:19], 0\n"
      "v_mfma_f32_16x16x32_bf16 v[120:123], v[248:251], v[16:19], 0\n"
      "v_mfma_f32_16x16x32_bf16 v[124:127], v[252:255], v[16:19], 0\n"
      "pc6_%=:\n"
      "s_waitcnt lgkmcnt(0)\n"
      "s_sub_u32 s58, s58, s67\n"
      "s_cmp_ge_i32 s58, 0\n"
      "s_cbranch_scc1 pd6_%=\n"
      "s_memrealtime s[58:59]\n"
      "s_branch pc6_%=\n"
      "pd6_%=:\n"
      "s_add_u32 s67, s67, 53\n"
      "buffer_load_dwordx4 v[224:227], v3, s[20:23], 0 offen nt\n"
      "buffer_load_dwordx4 v[228:231], v3, s[20:23], s36 offen nt\n"
      "buffer_load_dwordx4 v[232:235], v3, s[20:23], s37 offen nt\n"
      "buffer_load_dwordx4 v[236:239], v3, s[20:23], s38 offen nt\n"
      "buffer_load_dwordx4 v[240:243], v3, s[20:23], s39 offen nt\n"
      "buffer_load_dwordx4 v[244:247], v3, s[20:23], s40 offen nt\n"
      "buffer_load_dwordx4 v[248:251], v3, s[20:23], s41 offen nt\n"
      "buffer_load_dwordx4 v[252:255], v3, s[20:23], s42 offen nt\n"
      "v_add_u32 v3, 0x10000, v3\n"
      "v_add_f32_dpp v96, v97, v96 row_shl:4 row_mask:0xf bank_mask:0xf bound_ctrl:1\n"
      "v_add_f32_dpp v100, v101, v100 row_shl:4 row_mask:0xf bank_mask:0xf bound_ctrl:1\n"
      "v_add_f32_dpp v104, v105, v104 row_shl:4 row_mask:0xf bank_mask:0xf bound_ctrl:1\n"
      "v_add_f32_dpp v108, v109, v108 row_shl:4 row_mask:0xf bank_mask:0xf bound_ctrl:1\n"
      "v_add_f32_dpp v112, v113, v112 row_shl:4 row_mask:0xf bank_mask:0xf bound_ctrl:1\n"
      "v_add_f32_dpp v116, v117, v116 row_shl:4 row_mask:0xf bank_mask:0xf bound_ctrl:1\n"
      "v_add_f32_dpp v120, v121, v120 row_shl:4 row_mask:0xf bank_mask:0xf bound_ctrl:1\n"
      "v_add_f32_dpp v124, v125, v124 row_shl:4 row_mask:0xf bank_mask:0xf bound_ctrl:1\n"
      "v_add_f32_dpp v96, v98, v96 row_shl:8 row_mask:0xf bank_mask:0xf bound_ctrl:1\n"
      "v_add_f32_dpp v100, v102, v100 row_shl:8 row_mask:0xf bank_mask:0xf bound_ctrl:1\n"
      "v_add_f32_dpp v104, v106, v104 row_shl:8 row_mask:0xf bank_mask:0xf bound_ctrl:1\n"
      "v_add_f32_dpp v108, v110, v108 row_shl:8 row_mask:0xf bank_mask:0xf bound_ctrl:1\n"
      "v_add_f32_dpp v112, v114, v112 row_shl:8 row_mask:0xf bank_mask:0xf bound_ctrl:1\n"
      "v_add_f32_dpp v116, v118, v116 row_shl:8 row_mask:0xf bank_mask:0xf bound_ctrl:1\n"
      "v_add_f32_dpp v120, v122, v120 row_shl:8 row_mask:0xf bank_mask:0xf bound_ctrl:1\n"
      "v_add_f32_dpp v124, v126, v124 row_shl:8 row_mask:0xf bank_mask:0xf bound_ctrl:1\n"
      "v_add_f32_dpp v24, v99, v96 row_shl:12 row_mask:0xf bank_mask:0xf bound_ctrl:1\n"
      "v_add_f32_dpp v25, v103, v100 row_shl:12 row_mask:0xf bank_mask:0xf bound_ctrl:1\n"
      "v_add_f32_dpp v26, v107, v104 row_shl:12 row_mask:0xf bank_mask:0xf bound_ctrl:1\n"
      "v_add_f32_dpp v27, v111, v108 row_shl:12 row_mask:0xf bank_mask:0xf bound_ctrl:1\n"
      "v_add_f32_dpp v28, v115, v112 row_shl:12 row_mask:0xf bank_mask:0xf bound_ctrl:1\n"
      "v_add_f32_dpp v29, v119, v116 row_shl:12 row_mask:0xf bank_mask:0xf bound_ctrl:1\n"
      "v_add_f32_dpp v30, v123, v120 row_shl:12 row_mask:0xf bank_mask:0xf bound_ctrl:1\n"
      "v_add_f32_dpp v31, v127, v124 row_shl:12 row_mask:0xf bank_mask:0xf bound_ctrl:1\n"
      "s_mov_b64 exec, s[54:55]\n"
      "ds_write_b128 v5, v[24:27] offset:1536\n"
      "ds_write_b128 v5, v[28:31] offset:1552\n"
      "s_mov_b64 exec, -1\n"
      "v_max3_f32 v7, v7, v24, v25\n"
      "v_max3_f32 v7, v7, v26, v27\n"
      "v_max3_f32 v7, v7, v28, v29\n"
      "v_max3_f32 v7, v7, v30, v31\n"
      "v_add_u32 v5, 0x800, v5\n"
      "s_add_u32 s45, s45, 1\n"
      "s_cmp_lg_u32 s45, 8\n"
      "s_cbranch_scc1 kloop_%=\n"
      "s_branch trans_%=\n"
      "kclaim_%=:\n"
      "v_readfirstlane_b32 s52, v56\n"
      "v_readfirstlane_b32 s53, v57\n"
      "v_readfirstlane_b32 s48, v59\n"
      "s_add_u32 s52, s52, 1\n"
      "s_addc_u32 s53, s53, 0\n"
      "s_mul_i32 s64, s52, 0x9e3779b1\n"
      "s_xor_b32 s64, s64, 0x7f4a7c15\n"
      "s_mul_i32 s65, s52, 0x85ebca6b\n"
      "s_xor_b32 s65, s65, 0x5bd1e995\n"
      "s_mul_i32 s70, s52, 0xc2b2ae35\n"
      "s_xor_b32 s70, s70, 0x165667b1\n"
      "s_mul_i32 s71, s52, 0x27d4eb2f\n"
      "s_xor_b32 s71, s71, 0xd3a2646d\n"
      "s_xor_b32 s65, s65, s53\n"
      "s_mov_b32 s68, s67\n"
      "v_mov_b32 v12, v58\n"
      "v_mov_b32 v13, v59\n"
      "s_cmp_eq_u32 s48, s52\n"
      "s_cbranch_scc1 slot_%=\n"
      "s_lshl_b32 s47, s67, 16\n"
      "v_mov_b32 v56, s47\n"
      "v_mov_b32 v57, s52\n"
      "s_mov_b64 exec, 1\n"
      "global_atomic_cmpswap_x2 v[12:13], v9, v[56:59], s[50:51] offset:128 sc0\n"
      "s_mov_b64 exec, -1\n"
      "s_branch slot_%=\n"
      "kdist_%=:\n"
      "v_readfirstlane_b32 s47, v12\n"
      "v_readfirstlane_b32 s48, v13\n"
      "s_cmp_lg_u32 s48, s52\n"
      "s_cbranch_scc1 slot_%=\n"
      "s_lshr_b32 s47, s47, 16\n"
      "s_sub_u32 s47, s68, s47\n"
      "s_sext_i32_i16 s47, s47\n"
      "s_max_i32 s47, s47, 0\n"
      "s_min_i32 s49, s47, 300\n"
      "s_lshl_b32 s47, s49, 4\n"
      "s_cbranch_scc0 slot_%=\n"
      "v_mov_b32 v10, s47\n"
      "s_mov_b64 exec, 1\n"
      "global_atomic_add v12, v9, v10, s[50:51] offset:128 sc0\n"
      "s_mov_b64 exec, -1\n"
      "s_branch slot_%=\n"
      "kmean_%=:\n"
      "v_readfirstlane_b32 s47, v12\n"
      "s_bfe_u32 s47, s47, 0xc0004\n"
      "s_lshr_b32 s48, s47, 2\n"
      "s_sub_u32 s48, s48, 11\n"
      "s_lshr_b32 s47, s47, 3\n"
      "s_max_i32 s47, s47, s48\n"
      "s_sub_u32 s47, s47, s49\n"
      "s_max_i32 s48, s47, 0\n"
      "s_add_u32 s67, s67, s48\n"
      "s_min_i32 s47, s47, 0\n"
      "s_mul_i32 s47, s47, 93\n"
      "s_add_u32 s47, s47, 512\n"
      "s_ashr_i32 s27, s47, 10\n"
      "s_branch kgo_%=\n"
      "trans_%=:\n"
      "s_waitcnt lgkmcnt(0)\n"
      "ds_read_b128 v[24:27], v6\n"
      "ds_read_b128 v[28:31], v6 offset:16\n"
      "v_mov_b32 v21, v7\n"
      "s_nop 1\n"
      "v_permlane16_swap_b32 v7, v21\n"
      "v_max_f32 v7, v7, v21\n"
      "v_mov_b32 v21, v7\n"
      "s_nop 1\n"
      "v_permlane32_swap_b32 v7, v21\n"
      "v_max_f32 v7, v7, v21\n"
      "v_mul_f32 v7, s60, v7\n"
      "v_mov_b32 v8, 0\n"
      "s_cmp_lg_u32 s44, 0\n"
      "s_cbranch_scc1 noseed_%=\n"
      "v_mfma_f32_16x16x32_bf16 v[112:115], v[52:55], v[16:19], 0\n"
      "v_mov_b32 v96, v80\n"
      "v_mov_b32 v97, v81\n"
      "v_mov_b32 v98, v82\n"
      "v_mov_b32 v99, v83\n"
      "v_mov_b32 v100, v84\n"
      "v_mov_b32 v101, v85\n"
      "v_mov_b32 v102, v86\n"
      "v_mov_b32 v103, v87\n"
      "v_mov_b32 v104, v88\n"
      "v_mov_b32 v105, v89\n"
      "v_mov_b32 v106, v90\n"
      "v_mov_b32 v107, v91\n"
      "v_mov_b32 v108, v92\n"
      "v_mov_b32 v109, v93\n"
      "v_mov_b32 v110, v94\n"
      "v_mov_b32 v111, v95\n"
      "s_lshr_b32 s47, s43, 3\n"
      "s_and_b32 s47, s47, 1\n"
      "s_lshl_b32 s47, s47, 4\n"
      "s_lshr_b32 s63, s63, s47\n"
      "s_lshl_b32 s63, s63, 16\n"
      "v_add_f32_dpp v20, v113, v112 row_shl:4 row_mask:0xf bank_mask:0xf bound_ctrl:1\n"
      "s_nop 1\n"
      "v_add_f32_dpp v20, v114, v20 row_shl:8 row_mask:0xf bank_mask:0xf bound_ctrl:1\n"
      "s_nop 1\n"
      "v_add_f32_dpp v20, v115, v20 row_shl:12 row_mask:0xf bank_mask:0xf bound_ctrl:1\n"
      "v_sub_f32 v21, 1.0, s63\n"
      "v_mul_f32 v21, 0xce6e6b28, v21\n"
      "v_mul_f32 v20, 0x3db504f3, v20\n"
      "v_add_f32 v20, v20, v21\n"
      "v_mul_f32 v20, 0x3fb8aa3b, v20\n"
      "v_max_f32 v7, v7, v20\n"
      "v_sub_f32 v20, v20, v7\n"
      "v_exp_f32 v20, v20\n"
      "v_cmp_gt_u32 vcc, 16, %2\n"
      "s_nop 1\n"
      "v_cndmask_b32 v8, 0, v20, vcc\n"
      "v_lshlrev_b32 v21, 16, v96\n"
      "v_mul_f32 v64, v20, v21\n"
      "v_lshlrev_b32 v21, 16, v100\n"
      "v_mul_f32 v65, v20, v21\n"
      "v_lshlrev_b32 v21, 16, v104\n"
      "v_mul_f32 v66, v20, v21\n"
      "v_lshlrev_b32 v21, 16, v108\n"
      "v_mul_f32 v67, v20, v21\n"
      "v_and_b32 v21, 0xffff0000, v96\n"
      "v_mul_f32 v68, v20, v21\n"
      "v_and_b32 v21, 0xffff0000, v100\n"
      "v_mul_f32 v69, v20, v21\n"
      "v_and_b32 v21, 0xffff0000, v104\n"
      "v_mul_f32 v70, v20, v21\n"
      "v_and_b32 v21, 0xffff0000, v108\n"
      "v_mul_f32 v71, v20, v21\n"
      "v_lshlrev_b32 v21, 16, v97\n"
      "v_mul_f32 v72, v20, v21\n"
      "v_lshlrev_b32 v21, 16, v101\n"
      "v_mul_f32 v73, v20, v21\n"
      "v_lshlrev_b32 v21, 16, v105\n"
      "v_mul_f32 v74, v20, v21\n"
      "v_lshlrev_b32 v21, 16, v109\n"
      "v_mul_f32 v75, v20, v21\n"
      "v_and_b32 v21, 0xffff0000, v97\n"
      "v_mul_f32 v76, v20, v21\n"
      "v_and_b32 v21, 0xffff0000, v101\n"
      "v_mul_f32 v77, v20, v21\n"
      "v_and_b32 v21, 0xffff0000, v105\n"
      "v_mul_f32 v78, v20, v21\n"
      "v_and_b32 v21, 0xffff0000, v109\n"
      "v_mul_f32 v79, v20, v21\n"
      "v_lshlrev_b32 v21, 16, v98\n"
      "v_mul_f32 v80, v20, v21\n"
      "v_lshlrev_b32 v21, 16, v102\n"
      "v_mul_f32 v81, v20, v21\n"
      "v_lshlrev_b32 v21, 16, v106\n"
      "v_mul_f32 v82, v20, v21\n"
      "v_lshlrev_b32 v21, 16, v110\n"
      "v_mul_f32 v83, v20, v21\n"
      "v_and_b32 v21, 0xffff0000, v98\n"
      "v_mul_f32 v84, v20, v21\n"
      "v_and_b32 v21, 0xffff0000, v102\n"
      "v_mul_f32 v85, v20, v21\n"
      "v_and_b32 v21, 0xffff0000, v106\n"
      "v_mul_f32 v86, v20, v21\n"
      "v_and_b32 v21, 0xffff0000, v110\n"
      "v_mul_f32 v87, v20, v21\n"
      "v_lshlrev_b32 v21, 16, v99\n"
      "v_mul_f32 v88, v20, v21\n"
      "v_lshlrev_b32 v21, 16, v103\n"
      "v_mul_f32 v89, v20, v21\n"
      "v_lshlrev_b32 v21, 16, v107\n"
      "v_mul_f32 v90, v20, v21\n"
      "v_lshlrev_b32 v21, 16, v111\n"
      "v_mul_f32 v91, v20, v21\n"
      "v_and_b32 v21, 0xffff0000, v99\n"
      "v_mul_f32 v92, v20, v21\n"
      "v_and_b32 v21, 0xffff0000, v103\n"
      "v_mul_f32 v93, v20, v21\n"
      "v_and_b32 v21, 0xffff0000, v107\n"
      "v_mul_f32 v94, v20, v21\n"
      "v_and_b32 v21, 0xffff0000, v111\n"
      "v_mul_f32 v95, v20, v21\n"
      "s_branch seeded_%=\n"
      "noseed_%=:\n"
      "v_mov_b32 v64, 0\n"
      "v_mov_b32 v65, 0\n"
      "v_mov_b32 v66, 0\n"
      "v_mov_b32 v67, 0\n"
      "v_mov_b32 v68, 0\n"
      "v_mov_b32 v69, 0\n"
      "v_mov_b32 v70, 0\n"
      "v_mov_b32 v71, 0\n"
      "v_mov_b32 v72, 0\n"
      "v_mov_b32 v73, 0\n"
      "v_mov_b32 v74, 0\n"
      "v_mov_b32 v75, 0\n"
      "v_mov_b32 v76, 0\n"
      "v_mov_b32 v77, 0\n"
      "v_mov_b32 v78, 0\n"
      "v_mov_b32 v79, 0\n"
      "v_mov_b32 v80, 0\n"
      "v_mov_b32 v81, 0\n"
      "v_mov_b32 v82, 0\n"
      "v_mov_b32 v83, 0\n"
      "v_mov_b32 v84, 0\n"
      "v_mov_b32 v85, 0\n"
      "v_mov_b32 v86, 0\n"
      "v_mov_b32 v87, 0\n"
      "v_mov_b32 v88, 0\n"
      "v_mov_b32 v89, 0\n"
      "v_mov_b32 v90, 0\n"
      "v_mov_b32 v91, 0\n"
      "v_mov_b32 v92, 0\n"
      "v_mov_b32 v93, 0\n"
      "v_mov_b32 v94, 0\n"
      "v_mov_b32 v95, 0\n"
      "seeded_%=:\n"
      "s_mov_b32 s45, 7\n"
      "vloop_%=:\n"
      "s_waitcnt lgkmcnt(0)\n"
      "ds_read_b128 v[44:47], v6 offset:512\n"
      "ds_read_b128 v[48:51], v6 offset:528\n"
      "v_fma_f32 v24, v24, s60, -v7\n"
      "v_fma_f32 v25, v25, s60, -v7\n"
      "v_fma_f32 v26, v26, s60, -v7\n"
      "v_fma_f32 v27, v27, s60, -v7\n"
      "v_fma_f32 v28, v28, s60, -v7\n"
      "v_fma_f32 v29, v29, s60, -v7\n"
      "v_fma_f32 v30, v30, s60, -v7\n"
      "v_fma_f32 v31, v31, s60, -v7\n"
      "v_exp_f32 v24, v24\n"
      "v_exp_f32 v25, v25\n"
      "v_exp_f32 v26, v26\n"
      "v_exp_f32 v27, v27\n"
      "v_exp_f32 v28, v28\n"
      "v_exp_f32 v29, v29\n"
      "v_exp_f32 v30, v30\n"
      "v_exp_f32 v31, v31\n"
      "s_nop 0\n"
      "v_pk_add_f32 v[20:21], v[24:25], v[26:27]\n"
      "v_pk_add_f32 v[22:23], v[28:29], v[30:31]\n"
      "v_cvt_pk_bf16_f32 v32, v24, v25\n"
      "v_cvt_pk_bf16_f32 v33, v26, v27\n"
      "v_cvt_pk_bf16_f32 v34, v28, v29\n"
      "v_cvt_pk_bf16_f32 v35, v30, v31\n"
      "v_pk_add_f32 v[20:21], v[20:21], v[22:23]\n"
      "v_add_f32 v8, v8, v20\n"
      "v_add_f32 v8, v8, v21\n"
      "s_waitcnt vmcnt(24)\n"
      "s_memrealtime s[58:59]\n"
      "v_perm_b32 v36, v132, v128, s61\n"
      "v_perm_b32 v37, v140, v136, s61\n"
      "v_perm_b32 v38, v148, v144, s61\n"
      "v_perm_b32 v39, v156, v152, s61\n"
      "v_perm_b32 v40, v132, v128, s62\n"
      "v_perm_b32 v41, v140, v136, s62\n"
      "v_perm_b32 v42, v148, v144, s62\n"
      "v_perm_b32 v43, v156, v152, s62\n"
      "v_mfma_f32_16x16x32_bf16 v[64:67], v[36:39], v[32:35], v[64:67]\n"
      "v_perm_b32 v36, v133, v129, s61\n"
      "v_perm_b32 v37, v141, v137, s61\n"
      "v_perm_b32 v38, v149, v145, s61\n"
      "v_perm_b32 v39, v157, v153, s61\n"
      "v_mfma_f32_16x16x32_bf16 v[68:71], v[40:43], v[32:35], v[68:71]\n"
      "v_perm_b32 v40, v133, v129, s62\n"
      "v_perm_b32 v41, v141, v137, s62\n"
      "v_perm_b32 v42, v149, v145, s62\n"
      "v_perm_b32 v43, v157, v153, s62\n"
      "v_mfma_f32_16x16x32_bf16 v[72:75], v[36:39], v[32:35], v[72:75]\n"
      "v_perm_b32 v36, v134, v130, s61\n"
      "v_perm_b32 v37, v142, v138, s61\n"
      "v_perm_b32 v38, v150, v146, s61\n"
      "v_perm_b32 v39, v158, v154, s61\n"
      "v_mfma_f32_16x16x32_bf16 v[76:79], v[40:43], v[32:35], v[76:79]\n"
      "v_perm_b32 v40, v134, v130, s62\n"
      "v_perm_b32 v41, v142, v138, s62\n"
      "v_perm_b32 v42, v150, v146, s62\n"
      "v_perm_b32 v43, v158, v154, s62\n"
      "v_mfma_f32_16x16x32_bf16 v[80:83], v[36:39], v[32:35], v[80:83]\n"
      "v_perm_b32 v36, v135, v131, s61\n"
      "v_perm_b32 v37, v143, v139, s61\n"
      "v_perm_b32 v38, v151, v147, s61\n"
      "v_perm_b32 v39, v159, v155, s61\n"
      "v_mfma_f32_16x16x32_bf16 v[84:87], v[40:43], v[32:35], v[84:87]\n"
      "v_perm_b32 v40, v135, v131, s62\n"
      "v_perm_b32 v41, v143, v139, s62\n"
      "v_perm_b32 v42, v151, v147, s62\n"
      "v_perm_b32 v43, v159, v155, s62\n"
      "v_mfma_f32_16x16x32_bf16 v[88:91], v[36:39], v[32:35], v[88:91]\n"
      "s_nop 0\n"
      "v_mfma_f32_16x16x32_bf16 v[92:95], v[40:43], v[32:35], v[92:95]\n"
      "s_add_u32 s67, s67, s27\n"
      "pc7_%=:\n"
      "s_waitcnt lgkmcnt(0)\n"
      "s_sub_u32 s58, s58, s67\n"
      "s_cmp_ge_i32 s58, 0\n"
      "s_cbranch_scc1 pd7_%=\n"
      "s_memrealtime s[58:59]\n"
      "s_branch pc7_%=\n"
      "pd7_%=:\n"
      "s_add_u32 s67, s67, 53\n"
      "buffer_load_dwordx4 v[128:131], v3, s[20:23], 0 offen nt\n"
      "buffer_load_dwordx4 v[132:135], v3, s[20:23], s36 offen nt\n"
      "buffer_load_dwordx4 v[136:139], v3, s[20:23], s37 offen nt\n"
      "buffer_load_dwordx4 v[140:143], v3, s[20:23], s38 offen nt\n"
      "buffer_load_dwordx4 v[144:147], v3, s[20:23], s39 offen nt\n"
      "buffer_load_dwordx4 v[148:151], v3, s[20:23], s40 offen nt\n"
      "buffer_load_dwordx4 v[152:155], v3, s[20:23], s41 offen nt\n"
      "buffer_load_dwordx4 v[156:159], v3, s[20:23], s42 offen nt\n"
      "v_add_u32 v3, 0x10000, v3\n"
      "s_waitcnt lgkmcnt(0)\n"
      "ds_read_b128 v[24:27], v6 offset:1024\n"
      "ds_read_b128 v[28:31], v6 offset:1040\n"
      "v_fma_f32 v44, v44, s60, -v7\n"
      "v_fma_f32 v45, v45, s60, -v7\n"
      "v_fma_f32 v46, v46, s60, -v7\n"
      "v_fma_f32 v47, v47, s60, -v7\n"
      "v_fma_f32 v48, v48, s60, -v7\n"
      "v_fma_f32 v49, v49, s60, -v7\n"
      "v_fma_f32 v50, v50, s60, -v7\n"
      "v_fma_f32 v51, v51, s60, -v7\n"
      "v_exp_f32 v44, v44\n"
      "v_exp_f32 v45, v45\n"
      "v_exp_f32 v46, v46\n"
      "v_exp_f32 v47, v47\n"
      "v_exp_f32 v48, v48\n"
      "v_exp_f32 v49, v49\n"
      "v_exp_f32 v50, v50\n"
      "v_exp_f32 v51, v51\n"
      "s_nop 0\n"
      "v_pk_add_f32 v[20:21], v[44:45], v[46:47]\n"
      "v_pk_add_f32 v[22:23], v[48:49], v[50:51]\n"
      "v_cvt_pk_bf16_f32 v32, v44, v45\n"
      "v_cvt_pk_bf16_f32 v33, v46, v47\n"
      "v_cvt_pk_bf16_f32 v34, v48, v49\n"
      "v_cvt_pk_bf16_f32 v35, v50, v51\n"
      "v_pk_add_f32 v[20:21], v[20:21], v[22:23]\n"
      "v_add_f32 v8, v8, v20\n"
      "v_add_f32 v8, v8, v21\n"
      "s_waitcnt vmcnt(24)\n"
      "s_memrealtime s[58:59]\n"
      "v_perm_b32 v36, v164, v160, s61\n"
      "v_perm_b32 v37, v172, v168, s61\n"
      "v_perm_b32 v38, v180, v176, s61\n"
      "v_perm_b32 v39, v188, v184, s61\n"
      "v_perm_b32 v40, v164, v160, s62\n"
      "v_perm_b32 v41, v172, v168, s62\n"
      "v_perm_b32 v42, v180, v176, s62\n"
      "v_perm_b32 v43, v188, v184, s62\n"
      "v_mfma_f32_16x16x32_bf16 v[64:67], v[36:39], v[32:35], v[64:67]\n"
      "v_perm_b32 v36, v165, v161, s61\n"
      "v_perm_b32 v37, v173, v169, s61\n"
      "v_perm_b32 v38, v181, v177, s61\n"
      "v_perm_b32 v39, v189, v185, s61\n"
      "v_mfma_f32_16x16x32_bf16 v[68:71], v[40:43], v[32:35], v[68:71]\n"
      "v_perm_b32 v40, v165, v161, s62\n"
      "v_perm_b32 v41, v173, v169, s62\n"
      "v_perm_b32 v42, v181, v177, s62\n"
      "v_perm_b32 v43, v189, v185, s62\n"
      "v_mfma_f32_16x16x32_bf16 v[72:75], v[36:39], v[32:35], v[72:75]\n"
      "v_perm_b32 v36, v166, v162, s61\n"
      "v_perm_b32 v37, v174, v170, s61\n"
      "v_perm_b32 v38, v182, v178, s61\n"
      "v_perm_b32 v39, v190, v186, s61\n"
      "v_mfma_f32_16x16x32_bf16 v[76:79], v[40:43], v[32:35], v[76:79]\n"
      "v_perm_b32 v40, v166, v162, s62\n"
      "v_perm_b32 v41, v174, v170, s62\n"
      "v_perm_b32 v42, v182, v178, s62\n"
      "v_perm_b32 v43, v190, v186, s62\n"
      "v_mfma_f32_16x16x32_bf16 v[80:83], v[36:39], v[32:35], v[80:83]\n"
      "v_perm_b32 v36, v167, v163, s61\n"
      "v_perm_b32 v37, v175, v171, s61\n"
      "v_perm_b32 v38, v183, v179, s61\n"
      "v_perm_b32 v39, v191, v187, s61\n"
      "v_mfma_f32_16x16x32_bf16 v[84:87], v[40:43], v[32:35], v[84:87]\n"
      "v_perm_b32 v40, v167, v163, s62\n"
      "v_perm_b32 v41, v175, v171, s62\n"
      "v_perm_b32 v42, v183, v179, s62\n"
      "v_perm_b32 v43, v191, v187, s62\n"
      "v_mfma_f32_16x16x32_bf16 v[88:91], v[36:39], v[32:35], v[88:91]\n"
      "s_nop 0\n"
      "v_mfma_f32_16x16x32_bf16 v[92:95], v[40:43], v[32:35], v[92:95]\n"
      "pc8_%=:\n"
      "s_waitcnt lgkmcnt(0)\n"
      "s_sub_u32 s58, s58, s67\n"
      "s_cmp_ge_i32 s58, 0\n"
      "s_cbranch_scc1 pd8_%=\n"
      "s_memrealtime s[58:59]\n"
      "s_branch pc8_%=\n"
      "pd8_%=:\n"
      "s_add_u32 s67, s67, 53\n"
      "buffer_load_dwordx4 v[160:163], v3, s[20:23], 0 offen nt\n"
      "buffer_load_dwordx4 v[164:167], v3, s[20:23], s36 offen nt\n"
      "buffer_load_dwordx4 v[168:171], v3, s[20:23], s37 offen nt\n"
      "buffer_load_dwordx4 v[172:175], v3, s[20:23], s38 offen nt\n"
      "buffer_load_dwordx4 v[176:179], v3, s[20:23], s39 offen nt\n"
      "buffer_load_dwordx4 v[180:183], v3, s[20:23], s40 offen nt\n"
      "buffer_load_dwordx4 v[184:187], v3, s[20:23], s41 offen nt\n"
      "buffer_load_dwordx4 v[188:191], v3, s[20:23], s42 offen nt\n"
      "v_add_u32 v3, 0x10000, v3\n"
      "s_waitcnt lgkmcnt(0)\n"
      "ds_read_b128 v[44:47], v6 offset:1536\n"
      "ds_read_b128 v[48:51], v6 offset:1552\n"
      "v_fma_f32 v24, v24, s60, -v7\n"
      "v_fma_f32 v25, v25, s60, -v7\n"
      "v_fma_f32 v26, v26, s60, -v7\n"
      "v_fma_f32 v27, v27, s60, -v7\n"
      "v_fma_f32 v28, v28, s60, -v7\n"
      "v_fma_f32 v29, v29, s60, -v7\n"
      "v_fma_f32 v30, v30, s60, -v7\n"
      "v_fma_f32 v31, v31, s60, -v7\n"
      "v_exp_f32 v24, v24\n"
      "v_exp_f32 v25, v25\n"
      "v_exp_f32 v26, v26\n"
      "v_exp_f32 v27, v27\n"
      "v_exp_f32 v28, v28\n"
      "v_exp_f32 v29, v29\n"
      "v_exp_f32 v30, v30\n"
      "v_exp_f32 v31, v31\n"
      "s_nop 0\n"
      "v_pk_add_f32 v[20:21], v[24:25], v[26:27]\n"
      "v_pk_add_f32 v[22:23], v[28:29], v[30:31]\n"
      "v_cvt_pk_bf16_f32 v32, v24, v25\n"
      "v_cvt_pk_bf16_f32 v33, v26, v27\n"
      "v_cvt_pk_bf16_f32 v34, v28, v29\n"
      "v_cvt_pk_bf16_f32 v35, v30, v31\n"
      "v_pk_add_f32 v[20:21], v[20:21], v[22:23]\n"
      "v_add_f32 v8, v8, v20\n"
      "v_add_f32 v8, v8, v21\n"
      "s_waitcnt vmcnt(24)\n"
      "s_memrealtime s[58:59]\n"
      "v_perm_b32 v36, v196, v192, s61\n"
      "v_perm_b32 v37, v204, v200, s61\n"
      "v_perm_b32 v38, v212, v208, s61\n"
      "v_perm_b32 v39, v220, v216, s61\n"
      "v_perm_b32 v40, v196, v192, s62\n"
      "v_perm_b32 v41, v204, v200, s62\n"
      "v_perm_b32 v42, v212, v208, s62\n"
      "v_perm_b32 v43, v220, v216, s62\n"
      "v_mfma_f32_16x16x32_bf16 v[64:67], v[36:39], v[32:35], v[64:67]\n"
      "v_perm_b32 v36, v197, v193, s61\n"
      "v_perm_b32 v37, v205, v201, s61\n"
      "v_perm_b32 v38, v213, v209, s61\n"
      "v_perm_b32 v39, v221, v217, s61\n"
      "v_mfma_f32_16x16x32_bf16 v[68:71], v[40:43], v[32:35], v[68:71]\n"
      "v_perm_b32 v40, v197, v193, s62\n"
      "v_perm_b32 v41, v205, v201, s62\n"
      "v_perm_b32 v42, v213, v209, s62\n"
      "v_perm_b32 v43, v221, v217, s62\n"
      "v_mfma_f32_16x16x32_bf16 v[72:75], v[36:39], v[32:35], v[72:75]\n"
      "v_perm_b32 v36, v198, v194, s61\n"
      "v_perm_b32 v37, v206, v202, s61\n"
      "v_perm_b32 v38, v214, v210, s61\n"
      "v_perm_b32 v39, v222, v218, s61\n"
      "v_mfma_f32_16x16x32_bf16 v[76:79], v[40:43], v[32:35], v[76:79]\n"
      "v_perm_b32 v40, v198, v194, s62\n"
      "v_perm_b32 v41, v206, v202, s62\n"
      "v_perm_b32 v42, v214, v210, s62\n"
      "v_perm_b32 v43, v222, v218, s62\n"
      "v_mfma_f32_16x16x32_bf16 v[80:83], v[36:39], v[32:35], v[80:83]\n"
      "v_perm_b32 v36, v199, v195, s61\n"
      "v_perm_b32 v37, v207, v203, s61\n"
      "v_perm_b32 v38, v215, v211, s61\n"
      "v_perm_b32 v39, v223, v219, s61\n"
      "v_mfma_f32_16x16x32_bf16 v[84:87], v[40:43], v[32:35], v[84:87]\n"
      "v_perm_b32 v40, v199, v195, s62\n"
      "v_perm_b32 v41, v207, v203, s62\n"
      "v_perm_b32 v42, v215, v211, s62\n"
      "v_perm_b32 v43, v223, v219, s62\n"
      "v_mfma_f32_16x16x32_bf16 v[88:91], v[36:39], v[32:35], v[88:91]\n"
      "s_nop 0\n"
      "v_mfma_f32_16x16x32_bf16 v[92:95], v[40:43], v[32:35], v[92:95]\n"
      "pc9_%=:\n"
      "s_waitcnt lgkmcnt(0)\n"
      "s_sub_u32 s58, s58, s67\n"
      "s_cmp_ge_i32 s58, 0\n"
      "s_cbranch_scc1 pd9_%=\n"
      "s_memrealtime s[58:59]\n"
      "s_branch pc9_%=\n"
      "pd9_%=:\n"
      "s_add_u32 s67, s67, 53\n"
      "buffer_load_dwordx4 v[192:195], v3, s[20:23], 0 offen nt\n"
      "buffer_load_dwordx4 v[196:199], v3, s[20:23], s36 offen nt\n"
      "buffer_load_dwordx4 v[200:203], v3, s[20:23], s37 offen nt\n"
      "buffer_load_dwordx4 v[204:207], v3, s[20:23], s38 offen nt\n"
      "buffer_load_dwordx4 v[208:211], v3, s[20:23], s39 offen nt\n"
      "buffer_load_dwordx4 v[212:215], v3, s[20:23], s40 offen nt\n"
      "buffer_load_dwordx4 v[216:219], v3, s[20:23], s41 offen nt\n"
      "buffer_load_dwordx4 v[220:223], v3, s[20:23], s42 offen nt\n"
      "v_add_u32 v3, 0x10000, v3\n"
      "s_waitcnt lgkmcnt(0)\n"
      "ds_read_b128 v[24:27], v6 offset:2048\n"
      "ds_read_b128 v[28:31], v6 offset:2064\n"
      "v_fma_f32 v44, v44, s60, -v7\n"
      "v_fma_f32 v45, v45, s60, -v7\n"
      "v_fma_f32 v46, v46, s60, -v7\n"
      "v_fma_f32 v47, v47, s60, -v7\n"
      "v_fma_f32 v48, v48, s60, -v7\n"
      "v_fma_f32 v49, v49, s60, -v7\n"
      "v_fma_f32 v50, v50, s60, -v7\n"
      "v_fma_f32 v51, v51, s60, -v7\n"
      "v_exp_f32 v44, v44\n"
      "v_exp_f32 v45, v45\n"
      "v_exp_f32 v46, v46\n"
      "v_exp_f32 v47, v47\n"
      "v_exp_f32 v48, v48\n"
      "v_exp_f32 v49, v49\n"
      "v_exp_f32 v50, v50\n"
      "v_exp_f32 v51, v51\n"
      "s_nop 0\n"
      "v_pk_add_f32 v[20:21], v[44:45], v[46:47]\n"
      "v_pk_add_f32 v[22:23], v[48:49], v[50:51]\n"
      "v_cvt_pk_bf16_f32 v32, v44, v45\n"
      "v_cvt_pk_bf16_f32 v33, v46, v47\n"
      "v_cvt_pk_bf16_f32 v34, v48, v49\n"
      "v_cvt_pk_bf16_f32 v35, v50, v51\n"
      "v_pk_add_f32 v[20:21], v[20:21], v[22:23]\n"
      "v_add_f32 v8, v8, v20\n"
      "v_add_f32 v8, v8, v21\n"
      "s_waitcnt vmcnt(24)\n"
      "s_memrealtime s[58:59]\n"
      "v_perm_b32 v36, v228, v224, s61\n"
      "v_perm_b32 v37, v236, v232, s61\n"
      "v_perm_b32 v38, v244, v240, s61\n"
      "v_perm_b32 v39, v252, v248, s61\n"
      "v_perm_b32 v40, v228, v224, s62\n"
      "v_perm_b32 v41, v236, v232, s62\n"
      "v_perm_b32 v42, v244, v240, s62\n"
      "v_perm_b32 v43, v252, v248, s62\n"
      "v_mfma_f32_16x16x32_bf16 v[64:67], v[36:39], v[32:35], v[64:67]\n"
      "v_perm_b32 v36, v229, v225, s61\n"
      "v_perm_b32 v37, v237, v233, s61\n"
      "v_perm_b32 v38, v245, v241, s61\n"
      "v_perm_b32 v39, v253, v249, s61\n"
      "v_mfma_f32_16x16x32_bf16 v[68:71], v[40:43], v[32:35], v[68:71]\n"
      "v_perm_b32 v40, v229, v225, s62\n"
      "v_perm_b32 v41, v237, v233, s62\n"
      "v_perm_b32 v42, v245, v241, s62\n"
      "v_perm_b32 v43, v253, v249, s62\n"
      "v_mfma_f32_16x16x32_bf16 v[72:75], v[36:39], v[32:35], v[72:75]\n"
      "v_perm_b32 v36, v230, v226, s61\n"
      "v_perm_b32 v37, v238, v234, s61\n"
      "v_perm_b32 v38, v246, v242, s61\n"
      "v_perm_b32 v39, v254, v250, s61\n"
      "v_mfma_f32_16x16x32_bf16 v[76:79], v[40:43], v[32:35], v[76:79]\n"
      "v_perm_b32 v40, v230, v226, s62\n"
      "v_perm_b32 v41, v238, v234, s62\n"
      "v_perm_b32 v42, v246, v242, s62\n"
      "v_perm_b32 v43, v254, v250, s62\n"
      "v_mfma_f32_16x16x32_bf16 v[80:83], v[36:39], v[32:35], v[80:83]\n"
      "v_perm_b32 v36, v231, v227, s61\n"
      "v_perm_b32 v37, v239, v235, s61\n"
      "v_perm_b32 v38, v247, v243, s61\n"
      "v_perm_b32 v39, v255, v251, s61\n"
      "v_mfma_f32_16x16x32_bf16 v[84:87], v[40:43], v[32:35], v[84:87]\n"
      "v_perm_b32 v40, v231, v227, s62\n"
      "v_perm_b32 v41, v239, v235, s62\n"
      "v_perm_b32 v42, v247, v243, s62\n"
      "v_perm_b32 v43, v255, v251, s62\n"
      "v_mfma_f32_16x16x32_bf16 v[88:91], v[36:39], v[32:35], v[88:91]\n"
      "s_nop 0\n"
      "v_mfma_f32_16x16x32_bf16 v[92:95], v[40:43], v[32:35], v[92:95]\n"
      "pc10_%=:\n"
      "s_waitcnt lgkmcnt(0)\n"
      "s_sub_u32 s58, s58, s67\n"
      "s_cmp_ge_i32 s58, 0\n"
      "s_cbranch_scc1 pd10_%=\n"
      "s_memrealtime s[58:59]\n"
      "s_branch pc10_%=\n"
      "pd10_%=:\n"
      "s_add_u32 s67, s67, 53\n"
      "buffer_load_dwordx4 v[224:227], v3, s[20:23], 0 offen nt\n"
      "buffer_load_dwordx4 v[228:231], v3, s[20:23], s36 offen nt\n"
      "buffer_load_dwordx4 v[232:235], v3, s[20:23], s37 offen nt\n"
      "buffer_load_dwordx4 v[236:239], v3, s[20:23], s38 offen nt\n"
      "buffer_load_dwordx4 v[240:243], v3, s[20:23], s39 offen nt\n"
      "buffer_load_dwordx4 v[244:247], v3, s[20:23], s40 offen nt\n"
      "buffer_load_dwordx4 v[248:251], v3, s[20:23], s41 offen nt\n"
      "buffer_load_dwordx4 v[252:255], v3, s[20:23], s42 offen nt\n"
      "v_add_u32 v3, 0x10000, v3\n"
      "v_add_u32 v6, 0x800, v6\n"
      "s_sub_u32 s45, s45, 1\n"
      "s_cmp_lg_u32 s45, 0\n"
      "s_cbranch_scc1 vloop_%=\n"
      "s_waitcnt lgkmcnt(0)\n"
      "ds_read_b128 v[44:47], v6 offset:512\n"
      "ds_read_b128 v[48:51], v6 offset:528\n"
      "v_fma_f32 v24, v24, s60, -v7\n"
      "v_fma_f32 v25, v25, s60, -v7\n"
      "v_fma_f32 v26, v26, s60, -v7\n"
      "v_fma_f32 v27, v27, s60, -v7\n"
      "v_fma_f32 v28, v28, s60, -v7\n"
      "v_fma_f32 v29, v29, s60, -v7\n"
      "v_fma_f32 v30, v30, s60, -v7\n"
      "v_fma_f32 v31, v31, s60, -v7\n"
      "v_exp_f32 v24, v24\n"
      "v_exp_f32 v25, v25\n"
      "v_exp_f32 v26, v26\n"
      "v_exp_f32 v27, v27\n"
      "v_exp_f32 v28, v28\n"
      "v_exp_f32 v29, v29\n"
      "v_exp_f32 v30, v30\n"
      "v_exp_f32 v31, v31\n"
      "s_nop 0\n"
      "v_pk_add_f32 v[20:21], v[24:25], v[26:27]\n"
      "v_pk_add_f32 v[22:23], v[28:29], v[30:31]\n"
      "v_cvt_pk_bf16_f32 v32, v24, v25\n"
      "v_cvt_pk_bf16_f32 v33, v26, v27\n"
      "v_cvt_pk_bf16_f32 v34, v28, v29\n"
      "v_cvt_pk_bf16_f32 v35, v30, v31\n"
      "v_pk_add_f32 v[20:21], v[20:21], v[22:23]\n"
      "v_add_f32 v8, v8, v20\n"
      "v_add_f32 v8, v8, v21\n"
      "s_waitcnt vmcnt(24)\n"
      "v_perm_b32 v36, v132, v128, s61\n"
      "v_perm_b32 v37, v140, v136, s61\n"
      "v_perm_b32 v38, v148, v144, s61\n"
      "v_perm_b32 v39, v156, v152, s61\n"
      "v_perm_b32 v40, v132, v128, s62\n"
      "v_perm_b32 v41, v140, v136, s62\n"
      "v_perm_b32 v42, v148, v144, s62\n"
      "v_perm_b32 v43, v156, v152, s62\n"
      "v_mfma_f32_16x16x32_bf16 v[64:67], v[36:39], v[32:35], v[64:67]\n"
      "v_perm_b32 v36, v133, v129, s61\n"
      "v_perm_b32 v37, v141, v137, s61\n"
      "v_perm_b32 v38, v149, v145, s61\n"
      "v_perm_b32 v39, v157, v153, s61\n"
      "v_mfma_f32_16x16x32_bf16 v[68:71], v[40:43], v[32:35], v[68:71]\n"
      "v_perm_b32 v40, v133, v129, s62\n"
      "v_perm_b32 v41, v141, v137, s62\n"
      "v_perm_b32 v42, v149, v145, s62\n"
      "v_perm_b32 v43, v157, v153, s62\n"
      "v_mfma_f32_16x16x32_bf16 v[72:75], v[36:39], v[32:35], v[72:75]\n"
      "v_perm_b32 v36, v134, v130, s61\n"
      "v_perm_b32 v37, v142, v138, s61\n"
      "v_perm_b32 v38, v150, v146, s61\n"
      "v_perm_b32 v39, v158, v154, s61\n"
      "v_mfma_f32_16x16x32_bf16 v[76:79], v[40:43], v[32:35], v[76:79]\n"
      "v_perm_b32 v40, v134, v130, s62\n"
      "v_perm_b32 v41, v142, v138, s62\n"
      "v_perm_b32 v42, v150, v146, s62\n"
      "v_perm_b32 v43, v158, v154, s62\n"
      "v_mfma_f32_16x16x32_bf16 v[80:83], v[36:39], v[32:35], v[80:83]\n"
      "v_perm_b32 v36, v135, v131, s61\n"
      "v_perm_b32 v37, v143, v139, s61\n"
      "v_perm_b32 v38, v151, v147, s61\n"
      "v_perm_b32 v39, v159, v155, s61\n"
      "v_mfma_f32_16x16x32_bf16 v[84:87], v[40:43], v[32:35], v[84:87]\n"
      "v_perm_b32 v40, v135, v131, s62\n"
      "v_perm_b32 v41, v143, v139, s62\n"
      "v_perm_b32 v42, v151, v147, s62\n"
      "v_perm_b32 v43, v159, v155, s62\n"
      "v_mfma_f32_16x16x32_bf16 v[88:91], v[36:39], v[32:35], v[88:91]\n"
      "s_nop 0\n"
      "v_mfma_f32_16x16x32_bf16 v[92:95], v[40:43], v[32:35], v[92:95]\n"
      "s_waitcnt lgkmcnt(0)\n"
      "ds_read_b128 v[24:27], v6 offset:1024\n"
      "ds_read_b128 v[28:31], v6 offset:1040\n"
      "v_fma_f32 v44, v44, s60, -v7\n"
      "v_fma_f32 v45, v45, s60, -v7\n"
      "v_fma_f32 v46, v46, s60, -v7\n"
      "v_fma_f32 v47, v47, s60, -v7\n"
      "v_fma_f32 v48, v48, s60, -v7\n"
      "v_fma_f32 v49, v49, s60, -v7\n"
      "v_fma_f32 v50, v50, s60, -v7\n"
      "v_fma_f32 v51, v51, s60, -v7\n"
      "v_exp_f32 v44, v44\n"
      "v_exp_f32 v45, v45\n"
      "v_exp_f32 v46, v46\n"
      "v_exp_f32 v47, v47\n"
      "v_exp_f32 v48, v48\n"
      "v_exp_f32 v49, v49\n"
      "v_exp_f32 v50, v50\n"
      "v_exp_f32 v51, v51\n"
      "s_nop 0\n"
      "v_pk_add_f32 v[20:21], v[44:45], v[46:47]\n"
      "v_pk_add_f32 v[22:23], v[48:49], v[50:51]\n"
      "v_cvt_pk_bf16_f32 v32, v44, v45\n"
      "v_cvt_pk_bf16_f32 v33, v46, v47\n"
      "v_cvt_pk_bf16_f32 v34, v48, v49\n"
      "v_cvt_pk_bf16_f32 v35, v50, v51\n"
      "v_pk_add_f32 v[20:21], v[20:21], v[22:23]\n"
      "v_add_f32 v8, v8, v20\n"
      "v_add_f32 v8, v8, v21\n"
      "s_waitcnt vmcnt(16)\n"
      "v_perm_b32 v36, v164, v160, s61\n"
      "v_perm_b32 v37, v172, v168, s61\n"
      "v_perm_b32 v38, v180, v176, s61\n"
      "v_perm_b32 v39, v188, v184, s61\n"
      "v_perm_b32 v40, v164, v160, s62\n"
      "v_perm_b32 v41, v172, v168, s62\n"
      "v_perm_b32 v42, v180, v176, s62\n"
      "v_perm_b32 v43, v188, v184, s62\n"
      "v_mfma_f32_16x16x32_bf16 v[64:67], v[36:39], v[32:35], v[64:67]\n"
      "v_perm_b32 v36, v165, v161, s61\n"
      "v_perm_b32 v37, v173, v169, s61\n"
      "v_perm_b32 v38, v181, v177, s61\n"
      "v_perm_b32 v39, v189, v185, s61\n"
      "v_mfma_f32_16x16x32_bf16 v[68:71], v[40:43], v[32:35], v[68:71]\n"
      "v_perm_b32 v40, v165, v161, s62\n"
      "v_perm_b32 v41, v173, v169, s62\n"
      "v_perm_b32 v42, v181, v177, s62\n"
      "v_perm_b32 v43, v189, v185, s62\n"
      "v_mfma_f32_16x16x32_bf16 v[72:75], v[36:39], v[32:35], v[72:75]\n"
      "v_perm_b32 v36, v166, v162, s61\n"
      "v_perm_b32 v37, v174, v170, s61\n"
      "v_perm_b32 v38, v182, v178, s61\n"
      "v_perm_b32 v39, v190, v186, s61\n"
      "v_mfma_f32_16x16x32_bf16 v[76:79], v[40:43], v[32:35], v[76:79]\n"
      "v_perm_b32 v40, v166, v162, s62\n"
      "v_perm_b32 v41, v174, v170, s62\n"
      "v_perm_b32 v42, v182, v178, s62\n"
      "v_perm_b32 v43, v190, v186, s62\n"
      "v_mfma_f32_16x16x32_bf16 v[80:83], v[36:39], v[32:35], v[80:83]\n"
      "v_perm_b32 v36, v167, v163, s61\n"
      "v_perm_b32 v37, v175, v171, s61\n"
      "v_perm_b32 v38, v183, v179, s61\n"
      "v_perm_b32 v39, v191, v187, s61\n"
      "v_mfma_f32_16x16x32_bf16 v[84:87], v[40:43], v[32:35], v[84:87]\n"
      "v_perm_b32 v40, v167, v163, s62\n"
      "v_perm_b32 v41, v175, v171, s62\n"
      "v_perm_b32 v42, v183, v179, s62\n"
      "v_perm_b32 v43, v191, v187, s62\n"
      "v_mfma_f32_16x16x32_bf16 v[88:91], v[36:39], v[32:35], v[88:91]\n"
      "s_nop 0\n"
      "v_mfma_f32_16x16x32_bf16 v[92:95], v[40:43], v[32:35], v[92:95]\n"
      "s_waitcnt lgkmcnt(0)\n"
      "ds_read_b128 v[44:47], v6 offset:1536\n"
      "ds_read_b128 v[48:51], v6 offset:1552\n"
      "v_fma_f32 v24, v24, s60, -v7\n"
      "v_fma_f32 v25, v25, s60, -v7\n"
      "v_fma_f32 v26, v26, s60, -v7\n"
      "v_fma_f32 v27, v27, s60, -v7\n"
      "v_fma_f32 v28, v28, s60, -v7\n"
      "v_fma_f32 v29, v29, s60, -v7\n"
      "v_fma_f32 v30, v30, s60, -v7\n"
      "v_fma_f32 v31, v31, s60, -v7\n"
      "v_exp_f32 v24, v24\n"
      "v_exp_f32 v25, v25\n"
      "v_exp_f32 v26, v26\n"
      "v_exp_f32 v27, v27\n"
      "v_exp_f32 v28, v28\n"
      "v_exp_f32 v29, v29\n"
      "v_exp_f32 v30, v30\n"
      "v_exp_f32 v31, v31\n"
      "s_nop 0\n"
      "v_pk_add_f32 v[20:21], v[24:25], v[26:27]\n"
      "v_pk_add_f32 v[22:23], v[28:29], v[30:31]\n"
      "v_cvt_pk_bf16_f32 v32, v24, v25\n"
      "v_cvt_pk_bf16_f32 v33, v26, v27\n"
      "v_cvt_pk_bf16_f32 v34, v28, v29\n"
      "v_cvt_pk_bf16_f32 v35, v30, v31\n"
      "v_pk_add_f32 v[20:21], v[20:21], v[22:23]\n"
      "v_add_f32 v8, v8, v20\n"
      "v_add_f32 v8, v8, v21\n"
      "s_waitcnt vmcnt(8)\n"
      "v_perm_b32 v36, v196, v192, s61\n"
      "v_perm_b32 v37, v204, v200, s61\n"
      "v_perm_b32 v38, v212, v208, s61\n"
      "v_perm_b32 v39, v220, v216, s61\n"
      "v_perm_b32 v40, v196, v192, s62\n"
      "v_perm_b32 v41, v204, v200, s62\n"
      "v_perm_b32 v42, v212, v208, s62\n"
      "v_perm_b32 v43, v220, v216, s62\n"
      "v_mfma_f32_16x16x32_bf16 v[64:67], v[36:39], v[32:35], v[64:67]\n"
      "v_perm_b32 v36, v197, v193, s61\n"
      "v_perm_b32 v37, v205, v201, s61\n"
      "v_perm_b32 v38, v213, v209, s61\n"
      "v_perm_b32 v39, v221, v217, s61\n"
      "v_mfma_f32_16x16x32_bf16 v[68:71], v[40:43], v[32:35], v[68:71]\n"
      "v_perm_b32 v40, v197, v193, s62\n"
      "v_perm_b32 v41, v205, v201, s62\n"
      "v_perm_b32 v42, v213, v209, s62\n"
      "v_perm_b32 v43, v221, v217, s62\n"
      "v_mfma_f32_16x16x32_bf16 v[72:75], v[36:39], v[32:35], v[72:75]\n"
      "v_perm_b32 v36, v198, v194, s61\n"
      "v_perm_b32 v37, v206, v202, s61\n"
      "v_perm_b32 v38, v214, v210, s61\n"
      "v_perm_b32 v39, v222, v218, s61\n"
      "v_mfma_f32_16x16x32_bf16 v[76:79], v[40:43], v[32:35], v[76:79]\n"
      "v_perm_b32 v40, v198, v194, s62\n"
      "v_perm_b32 v41, v206, v202, s62\n"
      "v_perm_b32 v42, v214, v210, s62\n"
      "v_perm_b32 v43, v222, v218, s62\n"
      "v_mfma_f32_16x16x32_bf16 v[80:83], v[36:39], v[32:35], v[80:83]\n"
      "v_perm_b32 v36, v199, v195, s61\n"
      "v_perm_b32 v37, v207, v203, s61\n"
      "v_perm_b32 v38, v215, v211, s61\n"
      "v_perm_b32 v39, v223, v219, s61\n"
      "v_mfma_f32_16x16x32_bf16 v[84:87], v[40:43], v[32:35], v[84:87]\n"
      "v_perm_b32 v40, v199, v195, s62\n"
      "v_perm_b32 v41, v207, v203, s62\n"
      "v_perm_b32 v42, v215, v211, s62\n"
      "v_perm_b32 v43, v223, v219, s62\n"
      "v_mfma_f32_16x16x32_bf16 v[88:91], v[36:39], v[32:35], v[88:91]\n"
      "s_nop 0\n"
      "v_mfma_f32_16x16x32_bf16 v[92:95], v[40:43], v[32:35], v[92:95]\n"
      "s_waitcnt lgkmcnt(0)\n"
      "v_fma_f32 v44, v44, s60, -v7\n"
      "v_fma_f32 v45, v45, s60, -v7\n"
      "v_fma_f32 v46, v46, s60, -v7\n"
      "v_fma_f32 v47, v47, s60, -v7\n"
      "v_fma_f32 v48, v48, s60, -v7\n"
      "v_fma_f32 v49, v49, s60, -v7\n"
      "v_fma_f32 v50, v50, s60, -v7\n"
      "v_fma_f32 v51, v51, s60, -v7\n"
      "v_exp_f32 v44, v44\n"
      "v_exp_f32 v45, v45\n"
      "v_exp_f32 v46, v46\n"
      "v_exp_f32 v47, v47\n"
      "v_exp_f32 v48, v48\n"
      "v_exp_f32 v49, v49\n"
      "v_exp_f32 v50, v50\n"
      "v_exp_f32 v51, v51\n"
      "s_nop 0\n"
      "v_pk_add_f32 v[20:21], v[44:45], v[46:47]\n"
      "v_pk_add_f32 v[22:23], v[48:49], v[50:51]\n"
      "v_cvt_pk_bf16_f32 v32, v44, v45\n"
      "v_cvt_pk_bf16_f32 v33, v46, v47\n"
      "v_cvt_pk_bf16_f32 v34, v48, v49\n"
      "v_cvt_pk_bf16_f32 v35, v50, v51\n"
      "v_pk_add_f32 v[20:21], v[20:21], v[22:23]\n"
      "v_add_f32 v8, v8, v20\n"
      "v_add_f32 v8, v8, v21\n"
      "v_mov_b32 v21, v8\n"
      "s_nop 1\n"
      "v_permlane16_swap_b32 v8, v21\n"
      "v_add_f32 v8, v8, v21\n"
      "v_mov_b32 v21, v8\n"
      "s_nop 1\n"
      "v_permlane32_swap_b32 v8, v21\n"
      "v_add_f32 v8, v8, v21\n"
      "v_mov_b32 v104, v7\n"
      "v_mov_b32 v105, v8\n"
      "v_mov_b32 v10, 1\n"
      "s_lshl_b32 s47, s44, 12\n"
      "v_mov_b32_dpp v104, v7 row_shr:4 row_mask:0xf bank_mask:0x2\n"
      "v_mov_b32_dpp v105, v8 row_shr:4 row_mask:0xf bank_mask:0x2\n"
      "s_nop 0\n"
      "v_mov_b32_dpp v104, v7 row_shr:8 row_mask:0xf bank_mask:0x4\n"
      "v_mov_b32_dpp v105, v8 row_shr:8 row_mask:0xf bank_mask:0x4\n"
      "s_nop 0\n"
      "v_mov_b32_dpp v104, v7 row_shr:12 row_mask:0xf bank_mask:0x8\n"
      "v_mov_b32_dpp v105, v8 row_shr:12 row_mask:0xf bank_mask:0x8\n"
      "s_nop 0\n"
      "v_xor_b32 v106, s64, v104\n"
      "v_xor_b32 v107, s65, v105\n"
      "s_waitcnt vmcnt(0)\n"
      "s_mov_b64 exec, 1\n"
      "global_atomic_add v12, v9, v10, s[50:51] offset:128 sc0\n"
      "s_mov_b64 exec, -1\n"
      "v_perm_b32 v36, v228, v224, s61\n"
      "v_perm_b32 v37, v236, v232, s61\n"
      "v_perm_b32 v38, v244, v240, s61\n"
      "v_perm_b32 v39, v252, v248, s61\n"
      "v_perm_b32 v40, v228, v224, s62\n"
      "v_perm_b32 v41, v236, v232, s62\n"
      "v_perm_b32 v42, v244, v240, s62\n"
      "v_perm_b32 v43, v252, v248, s62\n"
      "v_mfma_f32_16x16x32_bf16 v[64:67], v[36:39], v[32:35], v[64:67]\n"
      "v_perm_b32 v36, v229, v225, s61\n"
      "v_perm_b32 v37, v237, v233, s61\n"
      "v_perm_b32 v38, v245, v241, s61\n"
      "v_perm_b32 v39, v253, v249, s61\n"
      "v_mfma_f32_16x16x32_bf16 v[68:71], v[40:43], v[32:35], v[68:71]\n"
      "v_perm_b32 v40, v229, v225, s62\n"
      "v_perm_b32 v41, v237, v233, s62\n"
      "v_perm_b32 v42, v245, v241, s62\n"
      "v_perm_b32 v43, v253, v249, s62\n"
      "v_mfma_f32_16x16x32_bf16 v[72:75], v[36:39], v[32:35], v[72:75]\n"
      "v_perm_b32 v36, v230, v226, s61\n"
      "v_perm_b32 v37, v238, v234, s61\n"
      "v_perm_b32 v38, v246, v242, s61\n"
      "v_perm_b32 v39, v254, v250, s61\n"
      "v_mfma_f32_16x16x32_bf16 v[76:79], v[40:43], v[32:35], v[76:79]\n"
      "v_perm_b32 v40, v230, v226, s62\n"
      "v_perm_b32 v41, v238, v234, s62\n"
      "v_perm_b32 v42, v246, v242, s62\n"
      "v_perm_b32 v43, v254, v250, s62\n"
      "v_mfma_f32_16x16x32_bf16 v[80:83], v[36:39], v[32:35], v[80:83]\n"
      "v_perm_b32 v36, v231, v227, s61\n"
      "v_perm_b32 v37, v239, v235, s61\n"
      "v_perm_b32 v38, v247, v243, s61\n"
      "v_perm_b32 v39, v255, v251, s61\n"
      "v_mfma_f32_16x16x32_bf16 v[84:87], v[40:43], v[32:35], v[84:87]\n"
      "v_perm_b32 v40, v231, v227, s62\n"
      "v_perm_b32 v41, v239, v235, s62\n"
      "v_perm_b32 v42, v247, v243, s62\n"
      "v_perm_b32 v43, v255, v251, s62\n"
      "v_mfma_f32_16x16x32_bf16 v[88:91], v[36:39], v[32:35], v[88:91]\n"
      "s_nop 0\n"
      "v_mfma_f32_16x16x32_bf16 v[92:95], v[40:43], v[32:35], v[92:95]\n"
      "v_mov_b32 v96, v64\n"
      "v_mov_b32 v97, v68\n"
      "v_mov_b32 v98, v72\n"
      "v_mov_b32 v99, v76\n"
      "v_mov_b32_dpp v96, v65 row_shr:4 row_mask:0xf bank_mask:0x2\n"
      "v_mov_b32_dpp v97, v69 row_shr:4 row_mask:0xf bank_mask:0x2\n"
      "v_mov_b32_dpp v98, v73 row_shr:4 row_mask:0xf bank_mask:0x2\n"
      "v_mov_b32_dpp v99, v77 row_shr:4 row_mask:0xf bank_mask:0x2\n"
      "v_mov_b32_dpp v96, v66 row_shr:8 row_mask:0xf bank_mask:0x4\n"
      "v_mov_b32_dpp v97, v70 row_shr:8 row_mask:0xf bank_mask:0x4\n"
      "v_mov_b32_dpp v98, v74 row_shr:8 row_mask:0xf bank_mask:0x4\n"
      "v_mov_b32_dpp v99, v78 row_shr:8 row_mask:0xf bank_mask:0x4\n"
      "v_mov_b32_dpp v96, v67 row_shr:12 row_mask:0xf bank_mask:0x8\n"
      "v_mov_b32_dpp v97, v71 row_shr:12 row_mask:0xf bank_mask:0x8\n"
      "v_mov_b32_dpp v98, v75 row_shr:12 row_mask:0xf bank_mask:0x8\n"
      "v_mov_b32_dpp v99, v79 row_shr:12 row_mask:0xf bank_mask:0x8\n"
      "v_xor_b32 v96, s70, v96\n"
      "buffer_store_dwordx4 v[96:99], v2, s[28:31], s47 offen offset:0 sc1\n"
      "v_bitop3_b32 v106, v96, v98, v106 bitop3:0x96\n"
      "v_bitop3_b32 v107, v97, v99, v107 bitop3:0x96\n"
      "v_mov_b32 v100, v80\n"
      "v_mov_b32 v101, v84\n"
      "v_mov_b32 v102, v88\n"
      "v_mov_b32 v103, v92\n"
      "v_mov_b32_dpp v100, v81 row_shr:4 row_mask:0xf bank_mask:0x2\n"
      "v_mov_b32_dpp v101, v85 row_shr:4 row_mask:0xf bank_mask:0x2\n"
      "v_mov_b32_dpp v102, v89 row_shr:4 row_mask:0xf bank_mask:0x2\n"
      "v_mov_b32_dpp v103, v93 row_shr:4 row_mask:0xf bank_mask:0x2\n"
      "v_mov_b32_dpp v100, v82 row_shr:8 row_mask:0xf bank_mask:0x4\n"
      "v_mov_b32_dpp v101, v86 row_shr:8 row_mask:0xf bank_mask:0x4\n"
      "v_mov_b32_dpp v102, v90 row_shr:8 row_mask:0xf bank_mask:0x4\n"
      "v_mov_b32_dpp v103, v94 row_shr:8 row_mask:0xf bank_mask:0x4\n"
      "v_mov_b32_dpp v100, v83 row_shr:12 row_mask:0xf bank_mask:0x8\n"
      "v_mov_b32_dpp v101, v87 row_shr:12 row_mask:0xf bank_mask:0x8\n"
      "v_mov_b32_dpp v102, v91 row_shr:12 row_mask:0xf bank_mask:0x8\n"
      "v_mov_b32_dpp v103, v95 row_shr:12 row_mask:0xf bank_mask:0x8\n"
      "v_xor_b32 v101, s71, v101\n"
      "buffer_store_dwordx4 v[100:103], v2, s[28:31], s47 offen offset:1024 sc1\n"
      "v_bitop3_b32 v106, v100, v102, v106 bitop3:0x96\n"
      "v_bitop3_b32 v107, v101, v103, v107 bitop3:0x96\n"
      "buffer_store_dwordx4 v[104:107], v2, s[28:31], s47 offen offset:2048 sc1\n"
      "s_waitcnt vmcnt(3)\n"
      "v_readfirstlane_b32 s47, v12\n"
      "s_and_b32 s47, s47, 15\n"
      "s_cmp_lg_u32 s47, 7\n"
      "s_cbranch_scc1 done_%=\n"
      "poll_%=:\n"
      "buffer_load_dwordx4 v[136:139], v2, s[28:31], 0 offen offset:2048 sc1\n"
      "buffer_load_dwordx4 v[148:151], v2, s[28:31], s36 offen offset:2048 sc1\n"
      "buffer_load_dwordx4 v[160:163], v2, s[28:31], s37 offen offset:2048 sc1\n"
      "buffer_load_dwordx4 v[172:175], v2, s[28:31], s38 offen offset:2048 sc1\n"
      "buffer_load_dwordx4 v[184:187], v2, s[28:31], s39 offen offset:2048 sc1\n"
      "buffer_load_dwordx4 v[196:199], v2, s[28:31], s40 offen offset:2048 sc1\n"
      "buffer_load_dwordx4 v[208:211], v2, s[28:31], s41 offen offset:2048 sc1\n"
      "buffer_load_dwordx4 v[220:223], v2, s[28:31], s42 offen offset:2048 sc1\n"
      "buffer_load_dwordx4 v[128:131], v2, s[28:31], 0 offen offset:0 sc1\n"
      "buffer_load_dwordx4 v[132:135], v2, s[28:31], 0 offen offset:1024 sc1\n"
      "buffer_load_dwordx4 v[140:143], v2, s[28:31], s36 offen offset:0 sc1\n"
      "buffer_load_dwordx4 v[144:147], v2, s[28:31], s36 offen offset:1024 sc1\n"
      "buffer_load_dwordx4 v[152:155], v2, s[28:31], s37 offen offset:0 sc1\n"
      "buffer_load_dwordx4 v[156:159], v2, s[28:31], s37 offen offset:1024 sc1\n"
      "buffer_load_dwordx4 v[164:167], v2, s[28:31], s38 offen offset:0 sc1\n"
      "buffer_load_dwordx4 v[168:171], v2, s[28:31], s38 offen offset:1024 sc1\n"
      "buffer_load_dwordx4 v[176:179], v2, s[28:31], s39 offen offset:0 sc1\n"
      "buffer_load_dwordx4 v[180:183], v2, s[28:31], s39 offen offset:1024 sc1\n"
      "buffer_load_dwordx4 v[188:191], v2, s[28:31], s40 offen offset:0 sc1\n"
      "buffer_load_dwordx4 v[192:195], v2, s[28:31], s40 offen offset:1024 sc1\n"
      "buffer_load_dwordx4 v[200:203], v2, s[28:31], s41 offen offset:0 sc1\n"
      "buffer_load_dwordx4 v[204:207], v2, s[28:31], s41 offen offset:1024 sc1\n"
      "buffer_load_dwordx4 v[212:215], v2, s[28:31], s42 offen offset:0 sc1\n"
      "buffer_load_dwordx4 v[216:219], v2, s[28:31], s42 offen offset:1024 sc1\n"
      "v_mov_b32 v22, s52\n"
      "v_mov_b32 v23, s53\n"
      "s_mov_b64 exec, 1\n"
      "global_store_dwordx2 v9, v[22:23], s[50:51] sc1\n"
      "s_mov_b64 exec, -1\n"
      "s_waitcnt vmcnt(17)\n"
      "v_max3_f32 v20, v136, v148, v160\n"
      "v_max3_f32 v21, v172, v184, v196\n"
      "v_max3_f32 v20, v20, v208, v220\n"
      "v_max_f32 v20, v20, v21\n"
      "v_sub_f32 v112, v136, v20\n"
      "v_sub_f32 v114, v148, v20\n"
      "v_sub_f32 v116, v160, v20\n"
      "v_sub_f32 v118, v172, v20\n"
      "v_sub_f32 v120, v184, v20\n"
      "v_sub_f32 v122, v196, v20\n"
      "v_sub_f32 v124, v208, v20\n"
      "v_sub_f32 v126, v220, v20\n"
      "v_exp_f32 v112, v112\n"
      "v_exp_f32 v114, v114\n"
      "v_exp_f32 v116, v116\n"
      "v_exp_f32 v118, v118\n"
      "v_exp_f32 v120, v120\n"
      "v_exp_f32 v122, v122\n"
      "v_exp_f32 v124, v124\n"
      "v_exp_f32 v126, v126\n"
      "v_mul_f32 v21, v112, v137\n"
      "v_fmac_f32 v21, v114, v149\n"
      "v_fmac_f32 v21, v116, v161\n"
      "v_fmac_f32 v21, v118, v173\n"
      "v_fmac_f32 v21, v120, v185\n"
      "v_fmac_f32 v21, v122, v197\n"
      "v_fmac_f32 v21, v124, v209\n"
      "v_fmac_f32 v21, v126, v221\n"
      "v_rcp_f32 v14, v21\n"
      "v_bitop3_b32 v20, v136, v138, s64 bitop3:0x96\n"
      "v_bitop3_b32 v21, v137, v139, s65 bitop3:0x96\n"
      "s_waitcnt vmcnt(15)\n"
      "v_bitop3_b32 v20, v128, v130, v20 bitop3:0x96\n"
      "v_bitop3_b32 v21, v129, v131, v21 bitop3:0x96\n"
      "v_bitop3_b32 v20, v132, v134, v20 bitop3:0x96\n"
      "v_bitop3_b32 v21, v133, v135, v21 bitop3:0x96\n"
      "v_xor_b32 v128, s70, v128\n"
      "v_xor_b32 v133, s71, v133\n"
      "v_pk_mul_f32 v[96:97], v[128:129], v[112:113] op_sel_hi:[1,0]\n"
      "v_pk_mul_f32 v[98:99], v[130:131], v[112:113] op_sel_hi:[1,0]\n"
      "v_pk_mul_f32 v[100:101], v[132:133], v[112:113] op_sel_hi:[1,0]\n"
      "v_pk_mul_f32 v[102:103], v[134:135], v[112:113] op_sel_hi:[1,0]\n"
      "v_or_b32 v108, v20, v21\n"
      "v_bitop3_b32 v20, v148, v150, s64 bitop3:0x96\n"
      "v_bitop3_b32 v21, v149, v151, s65 bitop3:0x96\n"
      "s_waitcnt vmcnt(13)\n"
      "v_bitop3_b32 v20, v140, v142, v20 bitop3:0x96\n"
      "v_bitop3_b32 v21, v141, v143, v21 bitop3:0x96\n"
      "v_bitop3_b32 v20, v144, v146, v20 bitop3:0x96\n"
      "v_bitop3_b32 v21, v145, v147, v21 bitop3:0x96\n"
      "v_xor_b32 v140, s70, v140\n"
      "v_xor_b32 v145, s71, v145\n"
      "v_pk_fma_f32 v[96:97], v[140:141], v[114:115], v[96:97] op_sel_hi:[1,0,1]\n"
      "v_pk_fma_f32 v[98:99], v[142:143], v[114:115], v[98:99] op_sel_hi:[1,0,1]\n"
      "v_pk_fma_f32 v[100:101], v[144:145], v[114:115], v[100:101] op_sel_hi:[1,0,1]\n"
      "v_pk_fma_f32 v[102:103], v[146:147], v[114:115], v[102:103] op_sel_hi:[1,0,1]\n"
      "v_or3_b32 v108, v108, v20, v21\n"
      "v_bitop3_b32 v20, v160, v162, s64 bitop3:0x96\n"
      "v_bitop3_b32 v21, v161, v163, s65 bitop3:0x96\n"
      "s_waitcnt vmcnt(11)\n"
      "v_bitop3_b32 v20, v152, v154, v20 bitop3:0x96\n"
      "v_bitop3_b32 v21, v153, v155, v21 bitop3:0x96\n"
      "v_bitop3_b32 v20, v156, v158, v20 bitop3:0x96\n"
      "v_bitop3_b32 v21, v157, v159, v21 bitop3:0x96\n"
      "v_xor_b32 v152, s70, v152\n"
      "v_xor_b32 v157, s71, v157\n"
      "v_pk_fma_f32 v[96:97], v[152:153], v[116:117], v[96:97] op_sel_hi:[1,0,1]\n"
      "v_pk_fma_f32 v[98:99], v[154:155], v[116:117], v[98:99] op_sel_hi:[1,0,1]\n"
      "v_pk_fma_f32 v[100:101], v[156:157], v[116:117], v[100:101] op_sel_hi:[1,0,1]\n"
      "v_pk_fma_f32 v[102:103], v[158:159], v[116:117], v[102:103] op_sel_hi:[1,0,1]\n"
      "v_or3_b32 v108, v108, v20, v21\n"
      "v_bitop3_b32 v20, v172, v174, s64 bitop3:0x96\n"
      "v_bitop3_b32 v21, v173, v175, s65 bitop3:0x96\n"
      "s_waitcnt vmcnt(9)\n"
      "v_bitop3_b32 v20, v164, v166, v20 bitop3:0x96\n"
      "v_bitop3_b32 v21, v165, v167, v21 bitop3:0x96\n"
      "v_bitop3_b32 v20, v168, v170, v20 bitop3:0x96\n"
      "v_bitop3_b32 v21, v169, v171, v21 bitop3:0x96\n"
      "v_xor_b32 v164, s70, v164\n"
      "v_xor_b32 v169, s71, v169\n"
      "v_pk_fma_f32 v[96:97], v[164:165], v[118:119], v[96:97] op_sel_hi:[1,0,1]\n"
      "v_pk_fma_f32 v[98:99], v[166:167], v[118:119], v[98:99] op_sel_hi:[1,0,1]\n"
      "v_pk_fma_f32 v[100:101], v[168:169], v[118:119], v[100:101] op_sel_hi:[1,0,1]\n"
      "v_pk_fma_f32 v[102:103], v[170:171], v[118:119], v[102:103] op_sel_hi:[1,0,1]\n"
      "v_or3_b32 v108, v108, v20, v21\n"
      "v_bitop3_b32 v20, v184, v186, s64 bitop3:0x96\n"
      "v_bitop3_b32 v21, v185, v187, s65 bitop3:0x96\n"
      "s_waitcnt vmcnt(7)\n"
      "v_bitop3_b32 v20, v176, v178, v20 bitop3:0x96\n"
      "v_bitop3_b32 v21, v177, v179, v21 bitop3:0x96\n"
      "v_bitop3_b32 v20, v180, v182, v20 bitop3:0x96\n"
      "v_bitop3_b32 v21, v181, v183, v21 bitop3:0x96\n"
      "v_xor_b32 v176, s70, v176\n"
      "v_xor_b32 v181, s71, v181\n"
      "v_pk_fma_f32 v[96:97], v[176:177], v[120:121], v[96:97] op_sel_hi:[1,0,1]\n"
      "v_pk_fma_f32 v[98:99], v[178:179], v[120:121], v[98:99] op_sel_hi:[1,0,1]\n"
      "v_pk_fma_f32 v[100:101], v[180:181], v[120:121], v[100:101] op_sel_hi:[1,0,1]\n"
      "v_pk_fma_f32 v[102:103], v[182:183], v[120:121], v[102:103] op_sel_hi:[1,0,1]\n"
      "v_or3_b32 v108, v108, v20, v21\n"
      "v_bitop3_b32 v20, v196, v198, s64 bitop3:0x96\n"
      "v_bitop3_b32 v21, v197, v199, s65 bitop3:0x96\n"
      "s_waitcnt vmcnt(5)\n"
      "v_bitop3_b32 v20, v188, v190, v20 bitop3:0x96\n"
      "v_bitop3_b32 v21, v189, v191, v21 bitop3:0x96\n"
      "v_bitop3_b32 v20, v192, v194, v20 bitop3:0x96\n"
      "v_bitop3_b32 v21, v193, v195, v21 bitop3:0x96\n"
      "v_xor_b32 v188, s70, v188\n"
      "v_xor_b32 v193, s71, v193\n"
      "v_pk_fma_f32 v[96:97], v[188:189], v[122:123], v[96:97] op_sel_hi:[1,0,1]\n"
      "v_pk_fma_f32 v[98:99], v[190:191], v[122:123], v[98:99] op_sel_hi:[1,0,1]\n"
      "v_pk_fma_f32 v[100:101], v[192:193], v[122:123], v[100:101] op_sel_hi:[1,0,1]\n"
      "v_pk_fma_f32 v[102:103], v[194:195], v[122:123], v[102:103] op_sel_hi:[1,0,1]\n"
      "v_or3_b32 v108, v108, v20, v21\n"
      "v_bitop3_b32 v20, v208, v210, s64 bitop3:0x96\n"
      "v_bitop3_b32 v21, v209, v211, s65 bitop3:0x96\n"
      "s_waitcnt vmcnt(3)\n"
      "v_bitop3_b32 v20, v200, v202, v20 bitop3:0x96\n"
      "v_bitop3_b32 v21, v201, v203, v21 bitop3:0x96\n"
      "v_bitop3_b32 v20, v204, v206, v20 bitop3:0x96\n"
      "v_bitop3_b32 v21, v205, v207, v21 bitop3:0x96\n"
      "v_xor_b32 v200, s70, v200\n"
      "v_xor_b32 v205, s71, v205\n"
      "v_pk_fma_f32 v[96:97], v[200:201], v[124:125], v[96:97] op_sel_hi:[1,0,1]\n"
      "v_pk_fma_f32 v[98:99], v[202:203], v[124:125], v[98:99] op_sel_hi:[1,0,1]\n"
      "v_pk_fma_f32 v[100:101], v[204:205], v[124:125], v[100:101] op_sel_hi:[1,0,1]\n"
      "v_pk_fma_f32 v[102:103], v[206:207], v[124:125], v[102:103] op_sel_hi:[1,0,1]\n"
      "v_or3_b32 v108, v108, v20, v21\n"
      "v_bitop3_b32 v20, v220, v222, s64 bitop3:0x96\n"
      "v_bitop3_b32 v21, v221, v223, s65 bitop3:0x96\n"
      "s_waitcnt vmcnt(1)\n"
      "v_bitop3_b32 v20, v212, v214, v20 bitop3:0x96\n"
      "v_bitop3_b32 v21, v213, v215, v21 bitop3:0x96\n"
      "v_bitop3_b32 v20, v216, v218, v20 bitop3:0x96\n"
      "v_bitop3_b32 v21, v217, v219, v21 bitop3:0x96\n"
      "v_xor_b32 v212, s70, v212\n"
      "v_xor_b32 v217, s71, v217\n"
      "v_pk_fma_f32 v[96:97], v[212:213], v[126:127], v[96:97] op_sel_hi:[1,0,1]\n"
      "v_pk_fma_f32 v[98:99], v[214:215], v[126:127], v[98:99] op_sel_hi:[1,0,1]\n"
      "v_pk_fma_f32 v[100:101], v[216:217], v[126:127], v[100:101] op_sel_hi:[1,0,1]\n"
      "v_pk_fma_f32 v[102:103], v[218:219], v[126:127], v[102:103] op_sel_hi:[1,0,1]\n"
      "v_or3_b32 v108, v108, v20, v21\n"
      "v_cmp_ne_u32 vcc, 0, v108\n"
      "v_pk_mul_f32 v[96:97], v[96:97], v[14:15] op_sel_hi:[1,0]\n"
      "v_pk_mul_f32 v[98:99], v[98:99], v[14:15] op_sel_hi:[1,0]\n"
      "v_pk_mul_f32 v[100:101], v[100:101], v[14:15] op_sel_hi:[1,0]\n"
      "v_pk_mul_f32 v[102:103], v[102:103], v[14:15] op_sel_hi:[1,0]\n"
      "s_sub_u32 s66, s66, 1\n"
      "s_cbranch_scc1 good_%=\n"
      "s_cbranch_vccnz poll_%=\n"
      "good_%=:\n"
      "v_cvt_pk_bf16_f32 v32, v96, v97\n"
      "v_cvt_pk_bf16_f32 v33, v98, v99\n"
      "v_cvt_pk_bf16_f32 v34, v100, v101\n"
      "v_cvt_pk_bf16_f32 v35, v102, v103\n"
      "global_store_dwordx4 v4, v[32:35], s[18:19]\n"
      "done_%=:\n"
      :
      : "s"(__builtin_amdgcn_kernarg_segment_ptr()), "s"(blockIdx.x), "v"(threadIdx.x),
        "s"((unsigned)(size_t)(__attribute__((address_space(3))) float*)scores)
      : "v1", "v2", "v3", "v4", "v5", "v6", "v7", "v8", "v9", "v10", "v11", "v12", "v13", "v14", "v15", "v16", "v17", "v18", "v19", "v20", "v21",
        "v22", "v23", "v24", "v25", "v26", "v27", "v28", "v29", "v30", "v31", "v32", "v33", "v34", "v35", "v36", "v37", "v38", "v39", "v40", "v41",
        "v42", "v43", "v44", "v45", "v46", "v47", "v48", "v49", "v50", "v51", "v52", "v53", "v54", "v55", "v56", "v57", "v58", "v59", "v60", "v61",
        "v62", "v63", "v64", "v65", "v66", "v67", "v68", "v69", "v70", "v71", "v72", "v73", "v74", "v75", "v76", "v77", "v78", "v79", "v80", "v81",
        "v82", "v83", "v84", "v85", "v86", "v87", "v88", "v89", "v90", "v91", "v92", "v93", "v94", "v95", "v96", "v97", "v98", "v99", "v100",
        "v101", "v102", "v103", "v104", "v105", "v106", "v107", "v108", "v109", "v110", "v111", "v112", "v113", "v114", "v115", "v116", "v117",
        "v118", "v119", "v120", "v121", "v122", "v123", "v124", "v125", "v126", "v127", "v128", "v129", "v130", "v131", "v132", "v133", "v134",
        "v135", "v136", "v137", "v138", "v139", "v140", "v141", "v142", "v143", "v144", "v145", "v146", "v147", "v148", "v149", "v150", "v151",
        "v152", "v153", "v154", "v155", "v156", "v157", "v158", "v159", "v160", "v161", "v162", "v163", "v164", "v165", "v166", "v167", "v168",
        "v169", "v170", "v171", "v172", "v173", "v174", "v175", "v176", "v177", "v178", "v179", "v180", "v181", "v182", "v183", "v184", "v185",
        "v186", "v187", "v188", "v189", "v190", "v191", "v192", "v193", "v194", "v195", "v196", "v197", "v198", "v199", "v200", "v201", "v202",
        "v203", "v204", "v205", "v206", "v207", "v208", "v209", "v210", "v211", "v212", "v213", "v214", "v215", "v216", "v217", "v218", "v219",
        "v220", "v221", "v222", "v223", "v224", "v225", "v226", "v227", "v228", "v229", "v230", "v231", "v232", "v233", "v234", "v235", "v236",
        "v237", "v238", "v239", "v240", "v241", "v242", "v243", "v244", "v245", "v246", "v247", "v248", "v249", "v250", "v251", "v252", "v253",
        "v254", "v255", "s4", "s5", "s6", "s7", "s8", "s9", "s10", "s11", "s12", "s13", "s14", "s15", "s16", "s17", "s18", "s19", "s20", "s21",
        "s22", "s23", "s24", "s25", "s26", "s27", "s28", "s29", "s30", "s31", "s36", "s37", "s38", "s39", "s40", "s41", "s42", "s43", "s44", "s45",
        "s46", "s47", "s48", "s49", "s50", "s51", "s52", "s53", "s54", "s55", "s56", "s57", "s58", "s59", "s60", "s61", "s62", "s63", "s64", "s65",
        "s66", "s67", "s68", "s69", "s70", "s71", "vcc", "scc", "memory");
}

extern "C" void kernel_launch(void* const* d_in, const int* in_sizes, int n_in, void* d_out, int out_size, void* d_ws,
                              size_t ws_size, hipStream_t stream) {
  (void)in_sizes; (void)n_in; (void)out_size; (void)ws_size;
  attn_kernel<<<512, 64, 0, stream>>>(d_in[0], d_in[1], d_in[2], d_in[3], d_in[4], d_in[5], d_ws, d_out);
}
```
